```python
import torch

def _softmax(x, axis=-1):
    return torch.softmax(x, dim=axis)
def _gelu(x):
    return torch.nn.functional.gelu(x, approximate="tanh")
def _silu(x):
    return torch.nn.functional.silu(x)
def _sigmoid(x):
    return torch.sigmoid(x)
def _layernorm(x, axis=-1, eps=1e-5):
    m = x.mean(dim=axis, keepdim=True)
    v = x.var(dim=axis, keepdim=True, unbiased=False)
    return (x - m) / torch.sqrt(v + eps)
def _scatter_add(tgt, idx, src, axis=0):
    return tgt.clone().index_add_(axis, idx, src)
def _segment_sum(src, seg, n):
    out = torch.zeros((n,) + src.shape[1:], dtype=src.dtype)
    return out.index_add_(0, seg, src)
def _pad(x, pw):
    flat = tuple(v for pair in reversed(pw) for v in pair)
    return torch.nn.functional.pad(x, flat)

kernel_name = 'interleave_layers__sliding_block__dense_activation_relu__prompt'

def setup_inputs():

    _w = (lambda a: (lambda t: t.to(torch.bfloat16) if t.is_floating_point() else t)(torch.as_tensor(a)))
    def _A_setup():
        B, S, D = 1, 2048, 448
        return {
            "x": _w((lambda *s: torch.randn(*s, dtype=torch.bfloat16) * 0.05)(B, S, D)),
            "wqkv": _w((lambda *s: torch.randn(*s, dtype=torch.bfloat16) * 0.05)(D, 3 * D)),
            "wo": _w((lambda *s: torch.randn(*s, dtype=torch.bfloat16) * 0.05)(D, D)),
        }
    _A_in = _A_setup()

    def _B_setup():
        B, S, D, Dff = 1, 2048, 448, 1792
        return {
            "x": _w((lambda *s: torch.randn(*s, dtype=torch.bfloat16) * 0.05)(B, S, D)),
            "w1": _w((lambda *s: torch.randn(*s, dtype=torch.bfloat16) * 0.05)(D, Dff)),
            "w2": _w((lambda *s: torch.randn(*s, dtype=torch.bfloat16) * 0.05)(Dff, D)),
        }
    _B_in = _B_setup()

    return {
        "A_x": _A_in["x"],
        "A_wqkv": _A_in["wqkv"],
        "A_wo": _A_in["wo"],
        "B_x": _B_in["x"],
        "B_w1": _B_in["w1"],
        "B_w2": _B_in["w2"],
    }

def reference(A_x, A_wqkv, A_wo, B_x, B_w1, B_w2):
    def _A_ref(x, wqkv, wo):
        B, S, D = x.shape
        H = 16; Dh = D // H; W = 256
        qkv = (lambda x, *d: x.permute(*d))((x @ wqkv).reshape(B, S, 3, H, Dh), 2, 0, 3, 1, 4)
        q, k, v = qkv[0], qkv[1], qkv[2]
        scores = torch.einsum("bhqd,bhkd->bhqk", q, k) * (Dh ** -0.5)
        idx = torch.arange(S)
        mask = (idx[:, None] - idx[None, :] >= 0) & (idx[:, None] - idx[None, :] < W)
        scores = torch.where(mask[None, None], scores, -1e9)
        p = _softmax(scores, axis=-1)
        o = torch.einsum("bhqk,bhkd->bhqd", p, v)
        return (lambda x, *d: x.permute(*d))(o, 0, 2, 1, 3).reshape(B, S, D) @ wo

    def _B_ref(x, w1, w2):
        h = torch.einsum("bsd,df->bsf", x, w1)
        zero = torch.zeros_like(h)
        h = torch.maximum(h, zero)
        return torch.einsum("bsf,fd->bsd", h, w2)

    x = A_x
    x = x + _A_ref(_layernorm(x, axis=-1), A_wqkv, A_wo)
    x = x + _B_ref(_layernorm(x, axis=-1), B_w1, B_w2)
    x = x + _A_ref(_layernorm(x, axis=-1), A_wqkv, A_wo)
    x = x + _B_ref(_layernorm(x, axis=-1), B_w1, B_w2)
    return x
```

```cpp
#include <hip/hip_runtime.h>
#include <hip/hip_bf16.h>
#include <stdint.h>
typedef unsigned short u16;
static __device__ __forceinline__ float b2f(u16 b){ return __builtin_bit_cast(float,(unsigned)b<<16);}
static __device__ __forceinline__ u16 f2b(float f){ __hip_bfloat16 h = __float2bfloat16(f); return __builtin_bit_cast(u16,h);}
constexpr int S=2048,D=448,H=16,DH=28,W=256,F=1792;
__global__ void k_ln(const u16* x,u16* y){ int r=blockIdx.x; int t=threadIdx.x; float v[7]; float s=0; for(int i=0;i<7;i++){v[i]=b2f(x[r*D+t+64*i]); s+=v[i];}
 for(int o=32;o;o>>=1) s+=__shfl_xor(s,o); float m=s/D; float q=0; for(int i=0;i<7;i++){v[i]-=m;q+=v[i]*v[i];} for(int o=32;o;o>>=1) q+=__shfl_xor(q,o);
 float rs=1.0f/sqrtf(q/D+1e-5f); for(int i=0;i<7;i++) y[r*D+t+64*i]=f2b(v[i]*rs);}
template<int MODE> __global__ void k_gemm(const u16* A,const u16* B,const u16* res,u16* C,int N,int K){
 int n=blockIdx.x*64+threadIdx.x; int m=blockIdx.y; if(n>=N) return; float acc=0; for(int k=0;k<K;k++) acc+=b2f(A[m*K+k])*b2f(B[k*N+n]);
 if(MODE==1) acc=fmaxf(acc,0.f); if(MODE==2) acc+=b2f(res[m*N+n]); C[m*N+n]=f2b(acc);}
__global__ void k_attn(const u16* qkv,u16* o){ int q=blockIdx.x,h=blockIdx.y,t=threadIdx.x; __shared__ float p[W]; __shared__ float red[4];
 int k=q-t; float s=-1e30f; if(k>=0){ s=0; for(int d=0;d<DH;d++) s+=b2f(qkv[q*3*D+h*DH+d])*b2f(qkv[k*3*D+D+h*DH+d]); s*=0.1889822365046136f;}
 float m=s; for(int o2=32;o2;o2>>=1) m=fmaxf(m,__shfl_xor(m,o2)); if((t&63)==0) red[t>>6]=m; __syncthreads(); m=fmaxf(fmaxf(red[0],red[1]),fmaxf(red[2],red[3])); __syncthreads();
 float e=k>=0?__expf(s-m):0.f; p[t]=e; float l=e; for(int o2=32;o2;o2>>=1) l+=__shfl_xor(l,o2); if((t&63)==0) red[t>>6]=l; __syncthreads(); l=red[0]+red[1]+red[2]+red[3];
 if(t<DH){ float a=0; for(int j=0;j<W&&j<=q;j++) a+=p[j]*b2f(qkv[(q-j)*3*D+2*D+h*DH+t]); o[q*D+h*DH+t]=f2b(a/l);} }
extern "C" void kernel_launch(void* const* d_in, const int* in_sizes, int n_in, void* d_out, int out_size, void* d_ws, size_t ws_size, hipStream_t stream){
 const u16* x=(const u16*)d_in[0]; const u16* wqkv=(const u16*)d_in[1]; const u16* wo=(const u16*)d_in[2]; const u16* w1=(const u16*)d_in[4]; const u16* w2=(const u16*)d_in[5];
 u16* ws=(u16*)d_ws; u16* xn=ws; u16* qkv=xn+S*D; u16* o=qkv+S*3*D; u16* x1=o+S*D; u16* hh=x1+S*D; u16* x2=hh+S*F; u16* out=(u16*)d_out;
 const u16* cur=x;
 for(int l=0;l<2;l++){
  k_ln<<<S,64,0,stream>>>(cur,xn);
  k_gemm<0><<<dim3(21,S),64,0,stream>>>(xn,wqkv,nullptr,qkv,3*D,D);
  k_attn<<<dim3(S,H),256,0,stream>>>(qkv,o);
  k_gemm<2><<<dim3(7,S),64,0,stream>>>(o,wo,cur,x1,D,D);
  k_ln<<<S,64,0,stream>>>(x1,xn);
  k_gemm<1><<<dim3(28,S),64,0,stream>>>(xn,w1,nullptr,hh,F,D);
  u16* dst = l==0? x2: out;
  k_gemm<2><<<dim3(7,S),64,0,stream>>>(hh,w2,x1,dst,D,F);
  cur=x2;
 }
}
```

```cpp
#include <hip/hip_runtime.h>
#include <hip/hip_bf16.h>
#include <stdint.h>

typedef unsigned v4u __attribute__((ext_vector_type(4)));
typedef unsigned v2u __attribute__((ext_vector_type(2)));
typedef float f32x2 __attribute__((ext_vector_type(2)));
typedef float f32x4 __attribute__((ext_vector_type(4)));
typedef float f32x16 __attribute__((ext_vector_type(16)));
typedef short s16x8 __attribute__((ext_vector_type(8)));
typedef short s16x4 __attribute__((ext_vector_type(4)));
typedef __bf16 bf16x2 __attribute__((ext_vector_type(2)));
#define LAS __attribute__((address_space(3)))
#define GAS __attribute__((address_space(1)))
typedef GAS unsigned long long gu64;
typedef __amdgpu_buffer_rsrc_t rsrc_t;
#define RLX_AGENT __ATOMIC_RELAXED, __HIP_MEMORY_SCOPE_AGENT
#define SB() __builtin_amdgcn_sched_barrier(0)
#define BAR() asm volatile("s_waitcnt lgkmcnt(0)\n\ts_barrier" ::: "memory")
#define DRAIN() asm volatile("s_waitcnt vmcnt(0)" ::: "memory")

constexpr int S = 2048, D = 448, NH = 16, DH = 28, FF = 1792;
constexpr int KB = 1024;
constexpr int OFF_FLAG = 0;
constexpr int FLAG_BYTES = 2 * 5 * 256 * 128;
constexpr int OFF_DUMMY = 448 * KB;
constexpr int OFF_PROF = 456 * KB;
constexpr int OFF_WQ = 512 * KB;
constexpr int OFF_WO = OFF_WQ + 1344 * KB;
constexpr int OFF_W1 = OFF_WO + 448 * KB;
constexpr int OFF_W2 = OFF_W1 + 1792 * KB;
constexpr int OFF_X = OFF_W2 + 1568 * KB;
constexpr int XG = 60 * KB, XI = 32 * XG;
constexpr int OFF_KV = OFF_X + 4 * XI;
constexpr int OFF_O = OFF_KV + 2 * 4096 * KB;
constexpr int OFF_H = OFF_O + 2 * 2048 * KB;
constexpr int WS_END = OFF_H + 2 * 7168 * KB;
constexpr unsigned OOB = 0x40000000u;
constexpr int SC1 = 16;
constexpr float NEG = -1e30f;
enum { PH_KV = 0, PH_O = 1, PH_X1 = 2, PH_H = 3, PH_X2 = 4 };

constexpr int L_XS = 0;
constexpr int L_Q = 56 * KB;
constexpr int L_MG = 64 * KB;
constexpr int L_ST = 84 * KB;
constexpr int LDS_BYTES = 88 * KB;

__device__ __forceinline__ f32x4 mfma16(v4u a, v4u b, f32x4 c) {
  return __builtin_amdgcn_mfma_f32_16x16x32_bf16(__builtin_bit_cast(s16x8, a), __builtin_bit_cast(s16x8, b), c, 0, 0, 0);
}
__device__ __forceinline__ f32x16 mfma32(v4u a, v4u b, f32x16 c) {
  return __builtin_amdgcn_mfma_f32_32x32x16_bf16(__builtin_bit_cast(s16x8, a), __builtin_bit_cast(s16x8, b), c, 0, 0, 0);
}
__device__ __forceinline__ unsigned pk(float a, float b) {
  f32x2 v = {a, b};
  return __builtin_bit_cast(unsigned, __builtin_convertvector(v, bf16x2));
}
__device__ __forceinline__ float blo(unsigned u) { return __builtin_bit_cast(float, u << 16); }
__device__ __forceinline__ float bhi(unsigned u) { return __builtin_bit_cast(float, u & 0xFFFF0000u); }
__device__ __forceinline__ rsrc_t mk_rsrc(const void* p, int bytes) { return __builtin_amdgcn_make_buffer_rsrc((void*)p, 0, bytes, 0x00020000); }
template <int AUX> __device__ __forceinline__ v4u ldf(rsrc_t rs, unsigned vo, int soff, int i) {
  return __builtin_amdgcn_raw_buffer_load_b128(rs, vo + (i & 3) * KB, soff + (i >> 2) * 4 * KB, AUX);
}
__device__ __forceinline__ void st16(rsrc_t rs, v4u v, unsigned vo, int soff) { __builtin_amdgcn_raw_buffer_store_b128(v, rs, vo + soff, 0, SC1); }
__device__ __forceinline__ float xsum16_32(float x) {
  auto a = __builtin_amdgcn_permlane16_swap(__float_as_uint(x), __float_as_uint(x), false, false);
  x = __uint_as_float(a[0]) + __uint_as_float(a[1]);
  auto b = __builtin_amdgcn_permlane32_swap(__float_as_uint(x), __float_as_uint(x), false, false);
  return __uint_as_float(b[0]) + __uint_as_float(b[1]);
}
__device__ __forceinline__ float xmax32(float x) {
  auto b = __builtin_amdgcn_permlane32_swap(__float_as_uint(x), __float_as_uint(x), false, false);
  return __builtin_fmaxf(__uint_as_float(b[0]), __uint_as_float(b[1]));
}

__device__ __forceinline__ void publish_x(rsrc_t rs, int xoff, int i, int w, int lane, const v2u (&res)[4]) {
  v4u c0 = {res[0].x, res[0].y, res[1].x, res[1].y}, c1 = {res[2].x, res[2].y, res[3].x, res[3].y};
  st16(rs, c0, lane * 16, xoff + (w * 14 + 2 * i) * KB);
  st16(rs, c1, lane * 16 + KB, xoff + (w * 14 + 2 * i) * KB);
  float s = 0.f, q = 0.f;
#pragma unroll
  for (int t = 0; t < 4; ++t) {
    const float a = blo(res[t].x), b = bhi(res[t].x), c = blo(res[t].y), d = bhi(res[t].y);
    s += (a + b) + (c + d);
    q += (a * a + b * b) + (c * c + d * d);
  }
  s = xsum16_32(s);
  q = xsum16_32(q);
  v2u sq = {__float_as_uint(s), __float_as_uint(q)};
  __builtin_amdgcn_raw_buffer_store_b64(sq, rs, lane < 16 ? lane * 8 : OOB, xoff + 56 * KB + i * 512 + w * 128, SC1);
}
__device__ __forceinline__ void load_res(const void* x, int g, int i, int w, int lane, v2u (&res)[4]) {
  const GAS char* p = (const GAS char*)x + (size_t)(64 * g + 16 * w + (lane & 15)) * (D * 2) + (64 * i + 4 * (lane >> 4)) * 2;
#pragma unroll
  for (int t = 0; t < 4; ++t) res[t] = *(const GAS v2u*)(p + 32 * t);
}

__global__ void __launch_bounds__(256) k_prep(const void* x, const void* wqkv, const void* wo, const void* w1, const void* w2, char* ws) {
  __shared__ __attribute__((aligned(16))) char img[4][2048];
  const int lane = threadIdx.x & 63, w = __builtin_amdgcn_readfirstlane(threadIdx.x >> 6);
  const int wid = blockIdx.x * 4 + w;
  const rsrc_t rs = mk_rsrc(ws, WS_END);
  if (threadIdx.x < 10) *(GAS unsigned long long*)(ws + OFF_FLAG + (threadIdx.x * 256 + blockIdx.x) * 128) = 0ull;
  if (blockIdx.x == 0) {
    v4u z = {0u, 0u, 0u, 0u};
    if (w == 1 && lane >= 32) z.z = pk(NEG, 0.f);
    *(GAS v4u*)(ws + OFF_DUMMY + threadIdx.x * 16) = z;
  }
  if (wid < 32 * 7 * 4) {
    const int g = wid / 28, i = (wid % 28) >> 2, ww = wid & 3;
    v2u res[4];
    load_res(x, g, i, ww, lane, res);
    publish_x(rs, OFF_X + g * XG, i, ww, lane, res);
  }
  LAS char* im = (LAS char*)&img[w][0];
  for (int u = wid; u < 2464; u += 1024) {
    const void* src;
    int bytes, pitch, row0, col0, dst0, dst1, kind;
    if (u < 672) {
      const int ks = u % 14, r = u / 14, p = r % 3, hh = r / 3;
      src = wqkv; bytes = D * 3 * D * 2; pitch = 3 * D * 2; row0 = 32 * ks;
      col0 = (p == 0 ? D : p == 1 ? 2 * D : 0) + DH * hh;
      dst0 = OFF_WQ + ((hh * 2) * 42 + ks * 3 + p) * KB; dst1 = dst0 + 42 * KB; kind = 1;
    } else if (u < 896) {
      const int v = u - 672, head = v & 15, m = (v >> 4) & 1, i = v >> 5;
      src = wo; bytes = D * D * 2; pitch = D * 2; row0 = DH * head; col0 = 64 * i + 32 * m;
      dst0 = OFF_WO + ((i * 4 + (head >> 2)) * 16 + (head & 3) * 4 + 2 * m) * KB; dst1 = dst0 + KB; kind = 2;
    } else if (u < 1680) {
      const int v = u - 896, ks = v % 14, r = v / 14, mm = r % 7, j = r / 7;
      src = w1; bytes = D * FF * 2; pitch = FF * 2; row0 = 32 * ks; col0 = 224 * j + 32 * mm;
      dst0 = OFF_W1 + ((j * 4 + (mm >> 1)) * 56 + ks * 4 + 2 * (mm & 1)) * KB; dst1 = dst0 + KB; kind = 0;
    } else {
      const int v = u - 1680, kap = v % 56, r = v / 56, m = r & 1, i = r >> 1;
      src = w2; bytes = D * FF * 2; pitch = D * 2; row0 = 32 * kap; col0 = 64 * i + 32 * m;
      dst0 = OFF_W2 + ((i * 4 + kap / 14) * 56 + (kap % 14) * 4 + 2 * m) * KB; dst1 = dst0 + KB; kind = 0;
    }
    const rsrc_t rw = mk_rsrc(src, bytes);
    const int c = lane & 3;
#pragma unroll
    for (int h = 0; h < 2; ++h) {
      const int rho = (lane >> 2) + 16 * h;
      unsigned vo = (row0 + rho) * pitch + col0 * 2 + c * 16;
      if (kind == 2 && rho >= DH) vo = OOB;
      v4u v = __builtin_amdgcn_raw_buffer_load_b128(rw, vo, 0, 0);
      if (kind == 1 && c == 3) { v.z = 0u; v.w = 0u; }
      *(LAS v4u*)(im + (c >> 1) * KB + rho * 32 + (c & 1) * 16) = v;
    }
    const int q = lane >> 4, qq = (lane & 15) >> 2, p4 = lane & 3;
    const int r0 = kind == 2 ? 16 * (q >> 1) + 4 * (q & 1) + qq : 4 * q + qq, rstep = kind == 2 ? 8 : 16;
#pragma unroll
    for (int t = 0; t < 2; ++t) {
      const s16x4 a = __builtin_amdgcn_ds_read_tr16_b64_v4i16((LAS s16x4*)(im + t * KB + r0 * 32 + p4 * 8));
      const s16x4 b = __builtin_amdgcn_ds_read_tr16_b64_v4i16((LAS s16x4*)(im + t * KB + (r0 + rstep) * 32 + p4 * 8));
      const v2u ua = __builtin_bit_cast(v2u, a), ub = __builtin_bit_cast(v2u, b);
      v4u f = {ua.x, ua.y, ub.x, ub.y};
      __builtin_amdgcn_raw_buffer_store_b128(f, rs, lane * 16 + (t ? dst1 : dst0), 0, 0);
    }
  }
}

#define STAMP(n)

__device__ __forceinline__ void publish(char* ws, int slot, int b) {
  DRAIN();
  BAR();
  if (threadIdx.x == 0) __hip_atomic_store((gu64*)(ws + OFF_FLAG + (slot * 256 + b) * 128), 1ull, RLX_AGENT);
}
__device__ __forceinline__ void await(char* ws, int slot, int w, int lane, int blk, bool active) {
  if (w == 0) {
    gu64* f = (gu64*)(ws + OFF_FLAG + (slot * 256 + blk) * 128);
    for (unsigned spin = 0; spin < (1u << 22); ++spin) {
      const bool ok = !active || __hip_atomic_load(f, RLX_AGENT) == 1ull;
      if (__all(ok)) break;
    }
    __builtin_amdgcn_fence(__ATOMIC_ACQUIRE, "wavefront");
  }
  BAR();
}
__device__ __forceinline__ int block_of(int g, int j) { return (((g & 3) * 8 + j) << 3) | (g >> 2); }

__device__ __forceinline__ void stage_issue(rsrc_t rs, int xoff, int w, int lane, v4u (&xa)[14], v2u (&sa)[2]) {
  const int q = lane >> 4;
  const unsigned so = q * 512 + (lane & 15) * 8;
  sa[0] = __builtin_amdgcn_raw_buffer_load_b64(rs, so, xoff + 56 * KB + w * 128, SC1);
  sa[1] = __builtin_amdgcn_raw_buffer_load_b64(rs, q < 3 ? so + 2048 : OOB, xoff + 56 * KB + w * 128, SC1);
#pragma unroll
  for (int ks = 0; ks < 14; ++ks) xa[ks] = ldf<SC1>(rs, lane * 16, xoff + w * 14 * KB, ks);
  SB();
}
__device__ __forceinline__ void stage_stats(LAS char* L, int w, int lane, const v2u (&sa)[2]) {
  const float s = xsum16_32(__uint_as_float(sa[0].x) + __uint_as_float(sa[1].x));
  const float q = xsum16_32(__uint_as_float(sa[0].y) + __uint_as_float(sa[1].y));
  const float mean = s * (1.0f / D);
  const float var = __builtin_fmaxf(q * (1.0f / D) - mean * mean, 0.f);
  const f32x2 mr = {mean, __builtin_amdgcn_rsqf(var + 1e-5f)};
  if (lane < 16) *(LAS f32x2*)(L + L_ST + (16 * w + lane) * 8) = mr;
}

__device__ __forceinline__ void reduce_res(LAS char* L, int w, int lane, f32x4 (&acc)[4][4], v2u (&res)[4]) {
#pragma unroll
  for (int d = 1; d < 4; ++d) {
    const int T = (w + d) & 3;
#pragma unroll
    for (int t = 0; t < 4; ++t) {
      const f32x4 v = T == 0 ? acc[t][0] : T == 1 ? acc[t][1] : T == 2 ? acc[t][2] : acc[t][3];
      *(LAS f32x4*)(L + L_XS + ((T * 3 + d - 1) * 4 + t) * KB + lane * 16) = v;
    }
  }
  f32x4 own[4];
#pragma unroll
  for (int t = 0; t < 4; ++t) own[t] = w == 0 ? acc[t][0] : w == 1 ? acc[t][1] : w == 2 ? acc[t][2] : acc[t][3];
  BAR();
#pragma unroll
  for (int t = 0; t < 4; ++t) {
    f32x4 v = own[t];
#pragma unroll
    for (int d = 0; d < 3; ++d) v += *(LAS f32x4*)(L + L_XS + ((w * 3 + d) * 4 + t) * KB + lane * 16);
    res[t].x = pk(blo(res[t].x) + v[0], bhi(res[t].x) + v[1]);
    res[t].y = pk(blo(res[t].y) + v[2], bhi(res[t].y) + v[3]);
  }
}

__global__ void __launch_bounds__(256, 1) k_main(const void* x, char* ws, void* out) {
  extern __shared__ __attribute__((aligned(16))) char smem[];
  LAS char* L = (LAS char*)smem;
  const int lane = threadIdx.x & 63, w = __builtin_amdgcn_readfirstlane(threadIdx.x >> 6);
  const int b = blockIdx.x, g = (b & 7) * 4 + (b >> 6), j = (b >> 3) & 7;
  const int tok = lane & 15, q = lane >> 4;
  const unsigned vo = lane * 16;
  const rsrc_t rs = mk_rsrc(ws, WS_END);
  const v4u ones = {0x3F803F80u, 0x3F803F80u, 0x3F803F80u, 0x3F803F80u};
  const f32x4 z4 = {0.f, 0.f, 0.f, 0.f};
  v2u res[4];
  load_res(x, g, j < 7 ? j : 0, w, lane, res);
  STAMP(0);

  for (int layer = 0; layer < 2; ++layer) {
    v4u wo_[16];
    {
      v4u wq[42];
      const int wqo = OFF_WQ + (j * 4 + w) * 42 * KB;
#pragma unroll
      for (int i = 0; i < 24; ++i) wq[i] = ldf<0>(rs, vo, wqo, i);
      SB();
      if (layer) await(ws, PH_X2, w, lane, block_of(g, lane), lane < 7);
      STAMP(1 + layer * 11);
      v4u xa[14];
      v2u sa[2];
      stage_issue(rs, OFF_X + layer * 2 * XI + g * XG, w, lane, xa, sa);
#pragma unroll
      for (int i = 24; i < 42; ++i) wq[i] = ldf<0>(rs, vo, wqo, i);
      SB();
      stage_stats(L, w, lane, sa);
#pragma unroll
      for (int ks = 0; ks < 7; ++ks) *(LAS v4u*)(L + L_XS + (ks * 4 + w) * KB + vo) = xa[ks];
      BAR();
      f32x4 ak[4], av[4], aq[4], ck = z4, cv = z4, cq = z4;
#pragma unroll
      for (int T = 0; T < 4; ++T) ak[T] = av[T] = aq[T] = z4;
      const int woo = OFF_WO + (j * 4 + w) * 16 * KB;
      const unsigned vo_wo = j < 7 ? vo : OOB;
#pragma unroll
      for (int ks = 0; ks < 14; ++ks) {
        if (ks == 4) {
#pragma unroll
          for (int k2 = 7; k2 < 14; ++k2) *(LAS v4u*)(L + L_XS + (k2 * 4 + w) * KB + vo) = xa[k2];
        }
        if (ks == 7) BAR();
        v4u bx[4];
#pragma unroll
        for (int T = 0; T < 4; ++T) bx[T] = *(LAS v4u*)(L + L_XS + (ks * 4 + T) * KB + vo);
        ck = mfma16(wq[ks * 3 + 0], ones, ck);
        cv = mfma16(ones, wq[ks * 3 + 1], cv);
        cq = mfma16(wq[ks * 3 + 2], ones, cq);
#pragma unroll
        for (int T = 0; T < 4; ++T) {
          ak[T] = mfma16(wq[ks * 3 + 0], bx[T], ak[T]);
          av[T] = mfma16(bx[T], wq[ks * 3 + 1], av[T]);
          aq[T] = mfma16(wq[ks * 3 + 2], bx[T], aq[T]);
        }
        if (ks >= 2 && ks < 10) {
          wo_[2 * (ks - 2)] = ldf<0>(rs, vo_wo, woo, 2 * (ks - 2));
          wo_[2 * (ks - 2) + 1] = ldf<0>(rs, vo_wo, woo, 2 * (ks - 2) + 1);
        }
        SB();
      }
      const int hl = w >> 1, t = w & 1, head = 2 * j + hl;
      const int kvo = OFF_KV + layer * 4096 * KB + (head * 64 + 2 * g) * 4 * KB;
      constexpr float QS = 0.18898223650461362f * 1.4426950408889634f;
      unsigned kp[4][2], qp[4][2];
#pragma unroll
      for (int T = 0; T < 4; ++T) {
        const f32x2 mr = *(LAS f32x2*)(L + L_ST + (16 * T + tok) * 8);
        float kv[4], qv[4];
#pragma unroll
        for (int i = 0; i < 4; ++i) {
          kv[i] = (ak[T][i] - mr.x * ck[i]) * mr.y;
          qv[i] = (aq[T][i] - mr.x * cq[i]) * (mr.y * QS);
        }
        if (t == 1 && q == 3) qv[0] = 1.0f;
        kp[T][0] = pk(kv[0], kv[1]); kp[T][1] = pk(kv[2], kv[3]);
        qp[T][0] = pk(qv[0], qv[1]); qp[T][1] = pk(qv[2], qv[3]);
      }
      const unsigned krow = (tok & 7) + 8 * (q & 1) + 16 * (tok >> 3) + 32 * (q >> 1);
      const unsigned qrow = tok + 16 * (q & 1) + 32 * (q >> 1);
      const unsigned vrow = (q >> 1) * KB + ((q & 1) * 32 + 16 * t + tok) * 16;
#pragma unroll
      for (int u = 0; u < 2; ++u) {
        auto k0 = __builtin_amdgcn_permlane16_swap(kp[2 * u][0], kp[2 * u + 1][0], false, false);
        auto k1 = __builtin_amdgcn_permlane16_swap(kp[2 * u][1], kp[2 * u + 1][1], false, false);
        v4u kc = {k0[0], k1[0], k0[1], k1[1]};
        st16(rs, kc, krow * 16, kvo + u * 4 * KB + t * KB);
        unsigned vp[4];
#pragma unroll
        for (int h = 0; h < 2; ++h) {
          const int T = 2 * u + h;
          const f32x4 m0 = *(LAS f32x4*)(L + L_ST + (16 * T + 4 * q) * 8), m1 = *(LAS f32x4*)(L + L_ST + (16 * T + 4 * q) * 8 + 16);
          float v0 = (av[T][0] - m0[0] * cv[0]) * m0[1], v1 = (av[T][1] - m0[2] * cv[0]) * m0[3];
          float v2 = (av[T][2] - m1[0] * cv[0]) * m1[1], v3 = (av[T][3] - m1[2] * cv[0]) * m1[3];
          if (t == 1 && tok == 12) v0 = v1 = v2 = v3 = 1.0f;
          vp[2 * h] = pk(v0, v1); vp[2 * h + 1] = pk(v2, v3);
        }
        v4u vc = {vp[0], vp[1], vp[2], vp[3]};
        st16(rs, vc, vrow, kvo + u * 4 * KB + 2 * KB);
        auto q0 = __builtin_amdgcn_permlane16_swap(qp[2 * u][0], qp[2 * u + 1][0], false, false);
        auto q1 = __builtin_amdgcn_permlane16_swap(qp[2 * u][1], qp[2 * u + 1][1], false, false);
        v4u qc = {q0[0], q1[0], q0[1], q1[1]};
        *(LAS v4u*)(L + L_Q + ((hl * 2 + u) * 2 + t) * KB + qrow * 16) = qc;
      }
      STAMP(2 + layer * 11);
      publish(ws, layer * 5 + PH_KV, b);
    }
    v4u w1_[56];
    const int w1o = OFF_W1 + (j * 4 + w) * 56 * KB;
    const unsigned vo_w1 = w < 3 ? vo : OOB;
    {
#pragma unroll
      for (int i = 0; i < 20; ++i) w1_[i] = ldf<0>(rs, (i & 2) ? vo_w1 : vo, w1o, i);
      SB();
      const int gg = g - 4 + lane;
      await(ws, layer * 5 + PH_KV, w, lane, block_of(gg & 31, j), lane < 4 && gg >= 0);
      STAMP(3 + layer * 11);
      const int hl = w >> 1, ab = w & 1, head = 2 * j + hl;
      v4u kf[5][2], vf[5][2];
      int tb[5];
#pragma unroll
      for (int i = 0; i < 5; ++i) {
        const int kt = 2 * g - 8 + (ab ? 9 - i : i);
        tb[i] = kt >= 0 ? OFF_KV + layer * 4096 * KB + (head * 64 + kt) * 4 * KB : OFF_DUMMY;
        kf[i][0] = ldf<SC1>(rs, vo, tb[i], 0);
        kf[i][1] = ldf<SC1>(rs, vo, tb[i], 1);
      }
#pragma unroll
      for (int i = 0; i < 5; ++i) {
        vf[i][0] = ldf<SC1>(rs, vo, tb[i], 2);
        vf[i][1] = ldf<SC1>(rs, vo, tb[i], 3);
      }
      SB();
      v4u qf[2][2];
#pragma unroll
      for (int x2 = 0; x2 < 2; ++x2)
#pragma unroll
        for (int s = 0; s < 2; ++s) qf[x2][s] = *(LAS v4u*)(L + L_Q + ((hl * 2 + (x2 ^ ab)) * 2 + s) * KB + vo);
      const int c = lane & 31, hh = lane >> 5;
      f32x16 mb, zz;
#pragma unroll
      for (int i = 0; i < 16; ++i) {
        const int key = (i & 3) + 4 * hh + 8 * (i >> 3) + 16 * ((i >> 2) & 1);
        mb[i] = ((key > c) != (ab != 0)) ? 0.f : NEG;
        zz[i] = 0.f;
      }
      constexpr int UT[9] = {0, 1, 1, 2, 2, 3, 3, 4, 4}, UX[9] = {0, 0, 1, 0, 1, 0, 1, 0, 1};
      float mx[2] = {-3e38f, -3e38f};
#pragma unroll
      for (int u = 0; u < 9; ++u) {
        f32x16 s = mfma32(kf[UT[u]][0], qf[UX[u]][0], (u == 0 || u == 2) ? mb : zz);
        s = mfma32(kf[UT[u]][1], qf[UX[u]][1], s);
        float m = mx[UX[u]];
#pragma unroll
        for (int i = 0; i < 16; ++i) m = __builtin_fmaxf(m, s[i]);
        mx[UX[u]] = m;
      }
      mx[0] = xmax32(mx[0]);
      mx[1] = xmax32(mx[1]);
      f32x16 nm[2], oa[2];
#pragma unroll
      for (int i = 0; i < 16; ++i) { nm[0][i] = -mx[0]; nm[1][i] = -mx[1]; oa[0][i] = 0.f; oa[1][i] = 0.f; }
#pragma unroll
      for (int u = 0; u < 9; ++u) {
        f32x16 s = mfma32(kf[UT[u]][0], qf[UX[u]][0], (u == 0 || u == 2) ? nm[UX[u]] + mb : nm[UX[u]]);
        s = mfma32(kf[UT[u]][1], qf[UX[u]][1], s);
        unsigned p[8];
#pragma unroll
        for (int i = 0; i < 8; ++i) p[i] = pk(__builtin_amdgcn_exp2f(s[2 * i]), __builtin_amdgcn_exp2f(s[2 * i + 1]));
        const v4u p0 = {p[0], p[1], p[2], p[3]}, p1 = {p[4], p[5], p[6], p[7]};
        oa[UX[u]] = mfma32(vf[UT[u]][0], p0, oa[UX[u]]);
        oa[UX[u]] = mfma32(vf[UT[u]][1], p1, oa[UX[u]]);
        if (u < 6) {
#pragma unroll
          for (int i = 20 + 4 * u; i < 24 + 4 * u; ++i) w1_[i] = ldf<0>(rs, (i & 2) ? vo_w1 : vo, w1o, i);
        }
      }
      LAS char* mine = L + L_MG + w * 5 * KB;
      LAS char* other = L + L_MG + (w ^ 1) * 5 * KB;
#pragma unroll
      for (int i = 0; i < 4; ++i) {
        const f32x4 v = {oa[1][4 * i], oa[1][4 * i + 1], oa[1][4 * i + 2], oa[1][4 * i + 3]};
        *(LAS f32x4*)(mine + i * KB + vo) = v;
      }
      *(LAS float*)(mine + 4 * KB + lane * 4) = mx[1];
      BAR();
      const float mo = *(LAS float*)(other + 4 * KB + lane * 4);
      const float mn = __builtin_fmaxf(mx[0], mo);
      const float f0 = __builtin_amdgcn_exp2f(mx[0] - mn), f1 = __builtin_amdgcn_exp2f(mo - mn);
      float o[16];
#pragma unroll
      for (int i = 0; i < 4; ++i) {
        const f32x4 v = *(LAS f32x4*)(other + i * KB + vo);
#pragma unroll
        for (int e = 0; e < 4; ++e) o[4 * i + e] = oa[0][4 * i + e] * f0 + v[e] * f1;
      }
      auto lsw = __builtin_amdgcn_permlane32_swap(__float_as_uint(o[12]), __float_as_uint(o[12]), false, false);
      const float rl = __builtin_amdgcn_rcpf(__uint_as_float(lsw[1]));
      const int oo = OFF_O + layer * 2048 * KB + ((g * 16 + head) * 4 + 2 * ab) * KB;
#pragma unroll
      for (int s = 0; s < 2; ++s) {
        v4u oc = {pk(o[8 * s] * rl, o[8 * s + 1] * rl), pk(o[8 * s + 2] * rl, o[8 * s + 3] * rl), pk(o[8 * s + 4] * rl, o[8 * s + 5] * rl),
                  pk(o[8 * s + 6] * rl, o[8 * s + 7] * rl)};
        st16(rs, oc, (c >> 4) * KB + ((2 * s + hh) * 16 + (c & 15)) * 16, oo);
      }
      STAMP(4 + layer * 11);
      publish(ws, layer * 5 + PH_O, b);
#pragma unroll
      for (int i = 44; i < 56; ++i) w1_[i] = ldf<0>(rs, (i & 2) ? vo_w1 : vo, w1o, i);
      SB();
    }
    if (j < 7) {
      await(ws, layer * 5 + PH_O, w, lane, block_of(g, lane), lane < 8);
      STAMP(5 + layer * 11);
      v4u oa[16];
      const int oo = OFF_O + layer * 2048 * KB + (g * 16 + 4 * w) * 4 * KB;
#pragma unroll
      for (int i = 0; i < 16; ++i) oa[i] = ldf<SC1>(rs, vo, oo, i);
      SB();
      f32x4 acc[4][4];
#pragma unroll
      for (int t = 0; t < 4; ++t)
#pragma unroll
        for (int T = 0; T < 4; ++T) acc[t][T] = z4;
#pragma unroll
      for (int ks = 0; ks < 4; ++ks)
#pragma unroll
        for (int T = 0; T < 4; ++T)
#pragma unroll
          for (int t = 0; t < 4; ++t) acc[t][T] = mfma16(wo_[ks * 4 + t], oa[ks * 4 + T], acc[t][T]);
      reduce_res(L, w, lane, acc, res);
      publish_x(rs, OFF_X + (layer * 2 + 1) * XI + g * XG, j, w, lane, res);
      STAMP(6 + layer * 11);
      publish(ws, layer * 5 + PH_X1, b);
    }
    v4u w2_[56];
    const int w2o = OFF_W2 + (j * 4 + w) * 56 * KB;
    const unsigned vo_w2 = j < 7 ? vo : OOB;
    {
      f32x4 cs[4] = {z4, z4, z4, z4};
#pragma unroll
      for (int ks = 0; ks < 14; ++ks)
#pragma unroll
        for (int t = 0; t < 4; ++t) cs[t] = mfma16(w1_[ks * 4 + t], ones, cs[t]);
      SB();
      await(ws, layer * 5 + PH_X1, w, lane, block_of(g, lane), lane < 7);
      STAMP(7 + layer * 11);
      v4u xa[14];
      v2u sa[2];
      stage_issue(rs, OFF_X + (layer * 2 + 1) * XI + g * XG, w, lane, xa, sa);
      stage_stats(L, w, lane, sa);
#pragma unroll
      for (int ks = 0; ks < 7; ++ks) *(LAS v4u*)(L + L_XS + (ks * 4 + w) * KB + vo) = xa[ks];
      BAR();
      f32x4 acc[4][4];
#pragma unroll
      for (int t = 0; t < 4; ++t)
#pragma unroll
        for (int T = 0; T < 4; ++T) acc[t][T] = z4;
#pragma unroll
      for (int ks = 0; ks < 14; ++ks) {
        if (ks == 3) {
#pragma unroll
          for (int k2 = 7; k2 < 14; ++k2) *(LAS v4u*)(L + L_XS + (k2 * 4 + w) * KB + vo) = xa[k2];
        }
        if (ks == 7) BAR();
        v4u bx[4];
#pragma unroll
        for (int T = 0; T < 4; ++T) bx[T] = *(LAS v4u*)(L + L_XS + (ks * 4 + T) * KB + vo);
#pragma unroll
        for (int T = 0; T < 4; ++T)
#pragma unroll
          for (int t = 0; t < 4; ++t) acc[t][T] = mfma16(w1_[ks * 4 + t], bx[T], acc[t][T]);
        if (ks < 11) {
#pragma unroll
          for (int t = 0; t < 4; ++t) w2_[ks * 4 + t] = ldf<0>(rs, vo_w2, w2o, ks * 4 + t);
        }
        SB();
      }
      const int ho = OFF_H + layer * 7168 * KB + (g * 56 + 7 * j + 2 * w) * 4 * KB;
#pragma unroll
      for (int T = 0; T < 4; ++T) {
        const f32x2 mr = *(LAS f32x2*)(L + L_ST + (16 * T + tok) * 8);
#pragma unroll
        for (int m = 0; m < 2; ++m) {
          float h[8];
#pragma unroll
          for (int i = 0; i < 8; ++i) h[i] = __builtin_fmaxf((acc[2 * m + (i >> 2)][T][i & 3] - mr.x * cs[2 * m + (i >> 2)][i & 3]) * mr.y, 0.f);
          v4u hc = {pk(h[0], h[1]), pk(h[2], h[3]), pk(h[4], h[5]), pk(h[6], h[7])};
          st16(rs, hc, (m ? vo_w1 : vo) + T * KB, ho + m * 4 * KB);
        }
      }
      STAMP(8 + layer * 11);
      publish(ws, layer * 5 + PH_H, b);
#pragma unroll
      for (int i = 44; i < 56; ++i) w2_[i] = ldf<0>(rs, vo_w2, w2o, i);
      SB();
    }
    if (j < 7) {
      await(ws, layer * 5 + PH_H, w, lane, block_of(g, lane), lane < 8);
      STAMP(9 + layer * 11);
      constexpr int WIN = 5;
      v4u ha[56];
      const int ho = OFF_H + layer * 7168 * KB + (g * 56 + 14 * w) * 4 * KB;
#pragma unroll
      for (int i = 0; i < 4 * WIN; ++i) ha[i] = ldf<SC1>(rs, vo, ho, i);
      SB();
      f32x4 acc[4][4];
#pragma unroll
      for (int t = 0; t < 4; ++t)
#pragma unroll
        for (int T = 0; T < 4; ++T) acc[t][T] = z4;
#pragma unroll
      for (int ks = 0; ks < 14; ++ks) {
#pragma unroll
        for (int T = 0; T < 4; ++T) {
#pragma unroll
          for (int t = 0; t < 4; ++t) acc[t][T] = mfma16(w2_[ks * 4 + t], ha[ks * 4 + T], acc[t][T]);
          if (ks + WIN < 14) ha[(ks + WIN) * 4 + T] = ldf<SC1>(rs, vo, ho, (ks + WIN) * 4 + T);
          SB();
        }
      }
      reduce_res(L, w, lane, acc, res);
      if (layer == 0) {
        publish_x(rs, OFF_X + 2 * XI + g * XG, j, w, lane, res);
        STAMP(10 + layer * 11);
        publish(ws, PH_X2, b);
      } else {
        GAS char* po = (GAS char*)out + (size_t)(64 * g + 16 * w + tok) * (D * 2) + (64 * j + 4 * q) * 2;
#pragma unroll
        for (int t = 0; t < 4; ++t) *(GAS v2u*)(po + 32 * t) = res[t];
        STAMP(10 + layer * 11);
      }
    }
    STAMP(11 + layer * 11);
  }
}

extern "C" void kernel_launch(void* const* d_in, const int* in_sizes, int n_in, void* d_out, int out_size, void* d_ws, size_t ws_size,
                              hipStream_t stream) {
  (void)hipFuncSetAttribute((const void*)k_main, hipFuncAttributeMaxDynamicSharedMemorySize, LDS_BYTES);
  k_prep<<<256, 256, 0, stream>>>(d_in[0], d_in[1], d_in[2], d_in[4], d_in[5], (char*)d_ws);
  k_main<<<256, 256, LDS_BYTES, stream>>>(d_in[0], (char*)d_ws, d_out);
}
```

```cpp
#include <hip/hip_runtime.h>
#include <hip/hip_bf16.h>

constexpr int LDS_BYTES = 150528;

__global__ void __launch_bounds__(256, 1) k_layers(const void* x, const void* wqkv, const void* wo, const void* w1, const void* w2, void* ws, void* out) {
  unsigned tid = threadIdx.x;
  asm volatile(
    "; prologue\n"
    "s_getpc_b64 s[62:63]\n"
    "Lpc_%=:\n"
    "s_mov_b32 s24, %8\n"
    "s_mov_b64 s[8:9], %6\n"
    "s_mov_b32 s10, 0x2908000\n"
    "s_mov_b32 s11, 0x20000\n"
    "s_mov_b32 s15, 0x20000\n"
    "v_lshrrev_b32 v1, 6, v0\n"
    "s_nop 0\n"
    "v_readfirstlane_b32 s20, v1\n"
    "v_and_b32 v0, 63, v0\n"
    "v_lshlrev_b32 v1, 4, v0\n"
    "s_and_b32 s54, s24, 7\n"
    "s_lshl_b32 s54, s54, 2\n"
    "s_lshr_b32 s21, s24, 6\n"
    "s_add_u32 s21, s21, s54\n"
    "s_bfe_u32 s22, s24, 0x30003\n"
    "s_mov_b32 s23, 0\n"
    "s_lshr_b32 s54, s24, 3\n"
    "s_lshl_b32 s54, s54, 2\n"
    "s_add_u32 s54, s54, s20\n"
    "s_lshl_b32 s54, s54, 8\n"
    "v_and_b32 v106, 1, v0\n"
    "v_lshl_add_u32 v106, v106, 7, s54\n"
    "v_min_u32 v106, (Lend_%=-Lpc_%=-128), v106\n"
    "global_load_dword v253, v106, s[62:63]\n"
    "s_mov_b32 s54, 0x7c000\n"
    "buffer_load_dwordx2 v[36:37], off, s[8:11], s54 sc1\n"
    "v_lshrrev_b32 v96, 2, v0\n"
    "v_and_b32 v97, 3, v0\n"
    "v_lshlrev_b32 v97, 4, v97\n"
    "s_mov_b32 s82, 0\n"
    "s_mov_b32 s83, 0\n"
    "s_mov_b32 s84, 0\n"
    "s_mov_b32 s85, 0\n"
    "s_lshl_b32 s54, s21, 2\n"
    "s_add_u32 s54, s54, s20\n"
    "s_cmp_ge_u32 s54, 0x54\n"
    "s_cbranch_scc1 Lkind_2_%=\n"
    "s_mul_i32 s57, s54, 0x124a\n"
    "s_lshr_b32 s57, s57, 16\n"
    "s_mul_i32 s58, s57, 14\n"
    "s_sub_u32 s58, s54, s58\n"
    "s_mul_i32 s59, s57, 0x5556\n"
    "s_lshr_b32 s59, s59, 16\n"
    "s_mul_i32 s60, s59, 3\n"
    "s_sub_u32 s60, s57, s60\n"
    "s_lshl_b32 s57, s22, 1\n"
    "s_add_u32 s57, s57, s59\n"
    "s_mul_i32 s55, s57, 56\n"
    "s_mul_i32 s57, s57, 0x15000\n"
    "s_add_u32 s66, s57, 0x100000\n"
    "s_mul_i32 s57, s58, 0x15000\n"
    "s_add_u32 s55, s55, s57\n"
    "s_add_u32 s57, s60, 1\n"
    "s_mul_i32 s57, s57, 0x380\n"
    "s_lshl_b32 s59, s58, 1\n"
    "s_add_u32 s59, s59, s60\n"
    "s_add_u32 s58, s58, 28\n"
    "s_cmp_eq_u32 s60, 2\n"
    "s_cselect_b32 s57, 0, s57\n"
    "s_cselect_b32 s59, s58, s59\n"
    "s_add_u32 s55, s55, s57\n"
    "s_lshl_b32 s59, s59, 10\n"
    "s_add_u32 s66, s66, s59\n"
    "s_mov_b64 s[12:13], %2\n"
    "s_mov_b32 s14, 0x126000\n"
    "s_mov_b32 s56, 0xa80\n"
    "s_add_u32 s67, s66, 0xa800\n"
    "s_mov_b32 s68, 512\n"
    "s_mov_b32 s69, 256\n"
    "s_mov_b32 s70, 128\n"
    "s_mov_b32 s82, 0x88888888\n"
    "s_mov_b32 s83, 0x88888888\n"
    "s_branch Lunit_1_%=\n"
    "Lkind_2_%=:\n"
    "s_cmp_ge_u32 s54, 0x74\n"
    "s_cbranch_scc1 Lkind_3_%=\n"
    "s_sub_u32 s54, s54, 84\n"
    "s_and_b32 s57, s54, 15\n"
    "s_lshr_b32 s58, s54, 4\n"
    "s_lshl_b32 s59, s22, 1\n"
    "s_add_u32 s59, s59, s58\n"
    "s_mul_i32 s55, s57, 0x6200\n"
    "s_lshl_b32 s60, s59, 6\n"
    "s_add_u32 s55, s55, s60\n"
    "s_lshl_b32 s59, s59, 5\n"
    "s_add_u32 s59, s59, s57\n"
    "s_lshl_b32 s59, s59, 10\n"
    "s_add_u32 s66, s59, 0x250000\n"
    "s_mov_b64 s[12:13], %3\n"
    "s_mov_b32 s14, 0x62000\n"
    "s_mov_b32 s56, 0x380\n"
    "s_add_u32 s67, s66, 0x4000\n"
    "s_mov_b32 s68, 512\n"
    "s_mov_b32 s69, 128\n"
    "s_mov_b32 s70, 256\n"
    "s_mov_b32 s85, 0xffff0000\n"
    "s_cmp_eq_u32 s22, 7\n"
    "s_cselect_b32 s14, 0, s14\n"
    "s_cselect_b32 s66, 0x40000000, s66\n"
    "s_cselect_b32 s67, 0x40000000, s67\n"
    "s_branch Lunit_1_%=\n"
    "Lkind_3_%=:\n"
    "s_sub_u32 s54, s54, 116\n"
    "s_mul_i32 s57, s54, 0x124a\n"
    "s_lshr_b32 s57, s57, 16\n"
    "s_mul_i32 s58, s57, 14\n"
    "s_sub_u32 s58, s54, s58\n"
    "s_mul_i32 s59, s22, 7\n"
    "s_add_u32 s59, s59, s57\n"
    "s_lshl_b32 s55, s59, 6\n"
    "s_mul_i32 s59, s58, 0x1c000\n"
    "s_add_u32 s55, s55, s59\n"
    "s_lshl_b32 s59, s22, 3\n"
    "s_add_u32 s59, s59, s57\n"
    "s_mul_i32 s59, s59, 14\n"
    "s_add_u32 s59, s59, s58\n"
    "s_lshl_b32 s59, s59, 11\n"
    "s_add_u32 s66, s59, 0x2c0000\n"
    "s_mov_b64 s[12:13], %4\n"
    "s_mov_b32 s14, 0x188000\n"
    "s_mov_b32 s56, 0xe00\n"
    "s_add_u32 s67, s66, 0x400\n"
    "s_mov_b32 s68, 512\n"
    "s_mov_b32 s69, 256\n"
    "s_mov_b32 s70, 128\n"
    "Lunit_1_%=:\n"
    "v_mul_u32_u24 v98, s56, v96\n"
    "v_add_u32 v98, v98, v97\n"
    "buffer_load_dwordx4 v[56:59], v98, s[12:15], s55 offen\n"
    "s_lshl_b32 s56, s56, 4\n"
    "v_add_u32 v98, s56, v98\n"
    "s_mov_b64 exec, s[84:85]\n"
    "v_mov_b32 v98, 0x40000000\n"
    "s_mov_b64 exec, -1\n"
    "buffer_load_dwordx4 v[60:63], v98, s[12:15], s55 offen\n"
    "s_mov_b64 s[12:13], %1\n"
    "s_mov_b32 s14, 0x1c0000\n"
    "v_and_b32 v106, 15, v0\n"
    "v_lshrrev_b32 v107, 4, v0\n"
    "v_mul_u32_u24 v108, 0x380, v106\n"
    "v_lshl_add_u32 v108, v107, 3, v108\n"
    "s_lshl_b32 s55, s22, 2\n"
    "s_add_u32 s55, s55, s20\n"
    "s_lshl_b32 s40, s55, 9\n"
    "s_mul_i32 s56, s21, 0xe000\n"
    "s_lshr_b32 s57, s40, 4\n"
    "s_add_u32 s56, s56, s57\n"
    "s_cmp_eq_u32 s22, 7\n"
    "s_cselect_b32 s56, 0x40000000, s56\n"
    "buffer_load_dwordx2 v[80:81], v108, s[12:15], s56 offen\n"
    "s_add_u32 s56, s56, 0x3800\n"
    "buffer_load_dwordx2 v[82:83], v108, s[12:15], s56 offen\n"
    "s_add_u32 s56, s56, 0x3800\n"
    "buffer_load_dwordx2 v[84:85], v108, s[12:15], s56 offen\n"
    "s_add_u32 s56, s56, 0x3800\n"
    "buffer_load_dwordx2 v[86:87], v108, s[12:15], s56 offen\n"
    "s_add_u32 s56, s56, 0x3800\n"
    "s_mul_i32 s28, s55, 0xa800\n"
    "s_add_u32 s28, s28, 0x100000\n"
    "s_mul_i32 s29, s55, 0x4000\n"
    "s_add_u32 s29, s29, 0x250000\n"
    "s_mul_i32 s30, s55, 0xe000\n"
    "s_add_u32 s30, s30, 0x2c0000\n"
    "s_mul_i32 s31, s55, 0xe000\n"
    "s_add_u32 s31, s31, 0x480000\n"
    "s_lshl_b32 s54, s21, 2\n"
    "s_add_u32 s54, s54, s20\n"
    "s_add_u32 s54, s54, 0x80\n"
    "s_cmp_ge_u32 s54, 0xd6\n"
    "s_cbranch_scc1 Lkind_6_%=\n"
    "s_sub_u32 s54, s54, 116\n"
    "s_mul_i32 s57, s54, 0x124a\n"
    "s_lshr_b32 s57, s57, 16\n"
    "s_mul_i32 s58, s57, 14\n"
    "s_sub_u32 s58, s54, s58\n"
    "s_mul_i32 s59, s22, 7\n"
    "s_add_u32 s59, s59, s57\n"
    "s_lshl_b32 s55, s59, 6\n"
    "s_mul_i32 s59, s58, 0x1c000\n"
    "s_add_u32 s55, s55, s59\n"
    "s_lshl_b32 s59, s22, 3\n"
    "s_add_u32 s59, s59, s57\n"
    "s_mul_i32 s59, s59, 14\n"
    "s_add_u32 s59, s59, s58\n"
    "s_lshl_b32 s59, s59, 11\n"
    "s_add_u32 s75, s59, 0x2c0000\n"
    "s_mov_b64 s[12:13], %4\n"
    "s_mov_b32 s14, 0x188000\n"
    "s_mov_b32 s56, 0xe00\n"
    "s_add_u32 s76, s75, 0x400\n"
    "s_mov_b32 s77, 512\n"
    "s_mov_b32 s78, 256\n"
    "s_mov_b32 s79, 128\n"
    "s_branch Lunit_5_%=\n"
    "Lkind_6_%=:\n"
    "s_sub_u32 s54, s54, 0xd6\n"
    "s_mul_i32 s57, s54, 0x493\n"
    "s_lshr_b32 s57, s57, 16\n"
    "s_mul_i32 s58, s57, 56\n"
    "s_sub_u32 s58, s54, s58\n"
    "s_lshl_b32 s59, s22, 1\n"
    "s_add_u32 s57, s57, s59\n"
    "s_lshl_b32 s55, s57, 6\n"
    "s_mul_i32 s59, s58, 0x7000\n"
    "s_add_u32 s55, s55, s59\n"
    "s_mul_i32 s57, s57, 0x1c000\n"
    "s_add_u32 s75, s57, 0x480000\n"
    "s_mul_i32 s59, s58, 0x2493\n"
    "s_lshr_b32 s59, s59, 16\n"
    "s_mul_i32 s60, s59, 7\n"
    "s_sub_u32 s60, s58, s60\n"
    "s_mul_i32 s57, s59, 3\n"
    "s_add_u32 s57, s57, 32\n"
    "s_lshl_b32 s59, s59, 2\n"
    "s_bitcmp1_b32 s60, 0\n"
    "s_cselect_b32 s57, s57, s59\n"
    "s_lshr_b32 s60, s60, 1\n"
    "s_add_u32 s57, s57, s60\n"
    "s_lshl_b32 s57, s57, 10\n"
    "s_add_u32 s75, s75, s57\n"
    "s_mov_b64 s[12:13], %5\n"
    "s_mov_b32 s14, 0x188000\n"
    "s_mov_b32 s56, 0x380\n"
    "s_add_u32 s76, s75, 0xe000\n"
    "s_mov_b32 s77, 256\n"
    "s_mov_b32 s78, 128\n"
    "s_mov_b32 s79, 512\n"
    "s_cmp_eq_u32 s22, 7\n"
    "s_cselect_b32 s14, 0, s14\n"
    "s_cselect_b32 s75, 0x40000000, s75\n"
    "s_cselect_b32 s76, 0x40000000, s76\n"
    "s_cmp_ge_u32 s54, 112\n"
    "s_cselect_b32 s14, 0, s14\n"
    "s_cselect_b32 s75, 0x40000000, s75\n"
    "s_cselect_b32 s76, 0x40000000, s76\n"
    "Lunit_5_%=:\n"
    "v_mul_u32_u24 v98, s56, v96\n"
    "v_add_u32 v98, v98, v97\n"
    "buffer_load_dwordx4 v[64:67], v98, s[12:15], s55 offen\n"
    "s_lshl_b32 s56, s56, 4\n"
    "v_add_u32 v98, s56, v98\n"
    "buffer_load_dwordx4 v[68:71], v98, s[12:15], s55 offen\n"
    "s_lshl_b32 s54, s21, 2\n"
    "s_add_u32 s54, s54, s20\n"
    "s_add_u32 s54, s54, 0x100\n"
    "s_sub_u32 s54, s54, 0xd6\n"
    "s_mul_i32 s57, s54, 0x493\n"
    "s_lshr_b32 s57, s57, 16\n"
    "s_mul_i32 s58, s57, 56\n"
    "s_sub_u32 s58, s54, s58\n"
    "s_lshl_b32 s59, s22, 1\n"
    "s_add_u32 s57, s57, s59\n"
    "s_lshl_b32 s55, s57, 6\n"
    "s_mul_i32 s59, s58, 0x7000\n"
    "s_add_u32 s55, s55, s59\n"
    "s_mul_i32 s57, s57, 0x1c000\n"
    "s_add_u32 s80, s57, 0x480000\n"
    "s_mul_i32 s59, s58, 0x2493\n"
    "s_lshr_b32 s59, s59, 16\n"
    "s_mul_i32 s60, s59, 7\n"
    "s_sub_u32 s60, s58, s60\n"
    "s_mul_i32 s57, s59, 3\n"
    "s_add_u32 s57, s57, 32\n"
    "s_lshl_b32 s59, s59, 2\n"
    "s_bitcmp1_b32 s60, 0\n"
    "s_cselect_b32 s57, s57, s59\n"
    "s_lshr_b32 s60, s60, 1\n"
    "s_add_u32 s57, s57, s60\n"
    "s_lshl_b32 s57, s57, 10\n"
    "s_add_u32 s80, s80, s57\n"
    "s_mov_b64 s[12:13], %5\n"
    "s_mov_b32 s14, 0x188000\n"
    "s_mov_b32 s56, 0x380\n"
    "s_add_u32 s81, s80, 0xe000\n"
    "s_mov_b32 s86, 256\n"
    "s_mov_b32 s87, 128\n"
    "s_mov_b32 s88, 512\n"
    "s_cmp_eq_u32 s22, 7\n"
    "s_cselect_b32 s14, 0, s14\n"
    "s_cselect_b32 s80, 0x40000000, s80\n"
    "s_cselect_b32 s81, 0x40000000, s81\n"
    "s_cmp_ge_u32 s54, 112\n"
    "s_cselect_b32 s14, 0, s14\n"
    "s_cselect_b32 s80, 0x40000000, s80\n"
    "s_cselect_b32 s81, 0x40000000, s81\n"
    "Lunit_8_%=:\n"
    "v_mul_u32_u24 v98, s56, v96\n"
    "v_add_u32 v98, v98, v97\n"
    "buffer_load_dwordx4 v[72:75], v98, s[12:15], s55 offen\n"
    "s_lshl_b32 s56, s56, 4\n"
    "v_add_u32 v98, s56, v98\n"
    "buffer_load_dwordx4 v[76:79], v98, s[12:15], s55 offen\n"
    "s_lshl_b32 s39, s24, 7\n"
    "v_mov_b32 v110, 0\n"
    "v_mov_b32 v111, 0\n"
    "v_mov_b32 v112, 0\n"
    "v_mov_b32 v113, 0\n"
    "v_lshrrev_b32 v109, 3, v0\n"
    "v_lshlrev_b32 v109, 15, v109\n"
    "v_and_b32 v114, 0x70, v1\n"
    "v_or_b32 v109, v109, v114\n"
    "s_cmp_lg_u32 s20, 0\n"
    "s_cbranch_scc1 Lskip_10_%=\n"
    "buffer_store_dwordx4 v[110:113], v109, s[8:11], s39 offen sc1\n"
    "s_add_u32 s54, s39, 0x40000\n"
    "s_mov_b32 exec_hi, 0\n"
    "buffer_store_dwordx4 v[110:113], v109, s[8:11], s54 offen sc1\n"
    "s_mov_b64 exec, -1\n"
    "Lskip_10_%=:\n"
    "s_mov_b64 s[16:17], %7\n"
    "s_mov_b32 s18, 0x1c0000\n"
    "s_mov_b32 s19, 0x20000\n"
    "s_mov_b32 s42, 0x3f803f80\n"
    "s_mov_b32 s50, 0x07060302\n"
    "s_mov_b32 s51, 0xffff0000\n"
    "s_mov_b32 s48, 0\n"
    "s_mov_b32 s49, -1\n"
    "s_mov_b32 s38, 0\n"
    "s_add_u32 s41, s40, 0xe000\n"
    "s_mul_i32 s74, s21, 0x12000\n"
    "s_add_u32 s74, s74, 0x608000\n"
    "v_mov_b32 v33, 0x40000000\n"
    "v_lshlrev_b32 v3, 3, v0\n"
    "v_bfe_u32 v109, v0, 4, 1\n"
    "v_lshrrev_b32 v115, 5, v0\n"
    "v_mul_u32_u24 v114, 0x3800, v109\n"
    "v_lshl_add_u32 v2, v115, 4, v106\n"
    "v_lshl_add_u32 v2, v2, 4, v114\n"
    "s_mul_i32 s54, s20, 0x1800\n"
    "s_add_u32 s54, s54, 0xe000\n"
    "v_lshl_add_u32 v99, v96, 5, s54\n"
    "v_bfe_u32 v114, v0, 1, 1\n"
    "v_lshl_add_u32 v99, v114, 10, v99\n"
    "v_and_b32 v114, 1, v0\n"
    "v_lshl_add_u32 v99, v114, 4, v99\n"
    "v_mov_b32 v100, v115\n"
    "v_mov_b32 v101, v109\n"
    "v_bfe_u32 v102, v0, 2, 2\n"
    "v_lshl_add_u32 v102, v102, 5, s54\n"
    "v_lshrrev_b32 v114, 1, v97\n"
    "v_add_u32 v102, v102, v114\n"
    "s_and_b32 s57, s24, 0xc7\n"
    "s_lshl_b32 s57, s57, 7\n"
    "v_and_b32 v114, 7, v0\n"
    "v_lshl_add_u32 v5, v114, 10, s57\n"
    "v_min_u32 v114, 6, v114\n"
    "v_lshl_add_u32 v4, v114, 10, s57\n"
    "s_lshl_b32 s57, s22, 3\n"
    "v_and_b32 v116, 3, v0\n"
    "v_bfe_u32 v35, v0, 2, 3\n"
    "v_lshl_add_u32 v35, v116, 6, v35\n"
    "v_add_u32 v35, s57, v35\n"
    "v_lshlrev_b32 v35, 7, v35\n"
    "s_sub_u32 s58, s21, 4\n"
    "v_and_b32 v114, 3, v0\n"
    "v_add_u32 v114, s58, v114\n"
    "v_max_i32 v114, 0, v114\n"
    "v_and_b32 v116, 3, v114\n"
    "v_bfe_u32 v40, v114, 2, 3\n"
    "v_lshl_add_u32 v40, v116, 6, v40\n"
    "v_add_u32 v40, s57, v40\n"
    "v_lshlrev_b32 v40, 7, v40\n"
    "v_mov_b32 v105, v35\n"
    "s_mov_b64 exec, s[48:49]\n"
    "v_mov_b32 v105, v4\n"
    "s_mov_b64 exec, -1\n"
    "s_waitcnt vmcnt(10)\n"
    "v_readfirstlane_b32 s54, v36\n"
    "v_readfirstlane_b32 s55, v37\n"
    "s_mul_hi_u32 s56, s54, 0x4c957f2d\n"
    "s_mul_i32 s55, s55, 0x4c957f2d\n"
    "s_add_u32 s56, s56, s55\n"
    "s_mul_i32 s55, s54, 0x5851f42d\n"
    "s_add_u32 s56, s56, s55\n"
    "s_mul_i32 s54, s54, 0x4c957f2d\n"
    "s_add_u32 s26, s54, 0xf767814f\n"
    "s_addc_u32 s27, s56, 0x14057b7e\n"
    "v_mov_b32 v36, s26\n"
    "v_mov_b32 v37, s27\n"
    "v_mov_b32 v38, s26\n"
    "v_mov_b32 v39, s27\n"
    "s_mov_b64 exec, s[82:83]\n"
    "s_waitcnt vmcnt(9)\n"
    "v_mov_b32 v58, 0\n"
    "v_mov_b32 v59, 0\n"
    "s_waitcnt vmcnt(8)\n"
    "v_mov_b32 v62, 0\n"
    "v_mov_b32 v63, 0\n"
    "s_mov_b64 exec, -1\n"
    "ds_write_b128 v99, v[56:59] offset:0\n"
    "ds_write_b128 v99, v[60:63] offset:512\n"
    "v_mad_u32_u24 v103, s68, v100, v102\n"
    "v_mad_u32_u24 v103, s69, v101, v103\n"
    "v_add_u32 v104, s70, v103\n"
    "ds_read_b64_tr_b16 v[56:57], v103 offset:0\n"
    "ds_read_b64_tr_b16 v[58:59], v104 offset:0\n"
    "ds_read_b64_tr_b16 v[60:61], v103 offset:1024\n"
    "ds_read_b64_tr_b16 v[62:63], v104 offset:1024\n"
    "s_waitcnt lgkmcnt(2)\n"
    "buffer_store_dwordx4 v[56:59], v1, s[8:11], s66 offen sc1\n"
    "s_waitcnt lgkmcnt(0)\n"
    "buffer_store_dwordx4 v[60:63], v1, s[8:11], s67 offen sc1\n"
    "s_cmp_eq_u32 s22, 7\n"
    "s_cbranch_scc1 Lskip_11_%=\n"
    "s_waitcnt vmcnt(9)\n"
    "v_dot2_f32_bf16 v88, s42, v80, 0\n"
    "v_dot2_f32_bf16 v92, v80, v80, 0\n"
    "s_waitcnt vmcnt(8)\n"
    "v_dot2_f32_bf16 v89, s42, v82, 0\n"
    "v_dot2_f32_bf16 v93, v82, v82, 0\n"
    "s_waitcnt vmcnt(7)\n"
    "v_dot2_f32_bf16 v90, s42, v84, 0\n"
    "v_dot2_f32_bf16 v94, v84, v84, 0\n"
    "s_waitcnt vmcnt(6)\n"
    "v_dot2_f32_bf16 v91, s42, v86, 0\n"
    "v_dot2_f32_bf16 v95, v86, v86, 0\n"
    "v_dot2_f32_bf16 v88, s42, v81, v88\n"
    "v_dot2_f32_bf16 v92, v81, v81, v92\n"
    "v_dot2_f32_bf16 v89, s42, v83, v89\n"
    "v_dot2_f32_bf16 v93, v83, v83, v93\n"
    "v_dot2_f32_bf16 v90, s42, v85, v90\n"
    "v_dot2_f32_bf16 v94, v85, v85, v94\n"
    "v_dot2_f32_bf16 v91, s42, v87, v91\n"
    "v_dot2_f32_bf16 v95, v87, v87, v95\n"
    "s_add_u32 s54, s74, s40\n"
    "s_add_u32 s55, s54, 0x7000\n"
    "s_add_u32 s56, s74, s41\n"
    "v_permlane16_swap_b32 v80, v82\n"
    "v_permlane16_swap_b32 v81, v83\n"
    "buffer_store_dwordx4 v[80:83], v2, s[8:11], s54 offen sc1\n"
    "v_permlane16_swap_b32 v84, v86\n"
    "v_permlane16_swap_b32 v85, v87\n"
    "buffer_store_dwordx4 v[84:87], v2, s[8:11], s55 offen sc1\n"
    "v_permlane16_swap_b32 v88, v89\n"
    "v_permlane16_swap_b32 v90, v91\n"
    "v_permlane16_swap_b32 v92, v93\n"
    "v_permlane16_swap_b32 v94, v95\n"
    "v_add_f32 v88, v88, v89\n"
    "v_add_f32 v90, v90, v91\n"
    "v_add_f32 v92, v92, v93\n"
    "v_add_f32 v94, v94, v95\n"
    "v_permlane32_swap_b32 v88, v90\n"
    "s_nop 0\n"
    "v_permlane32_swap_b32 v92, v94\n"
    "v_add_f32 v88, v88, v90\n"
    "v_add_f32 v89, v92, v94\n"
    "buffer_store_dwordx2 v[88:89], v3, s[8:11], s56 offen sc1\n"
    "Lskip_11_%=:\n"
    "s_cmp_lg_u32 s21, 0\n"
    "s_cbranch_scc1 Lskip_12_%=\n"
    "v_mov_b32 v110, 0\n"
    "v_mov_b32 v111, 0\n"
    "v_mov_b32 v112, 0\n"
    "v_mov_b32 v113, 0\n"
    "s_cmp_lg_u32 s20, 1\n"
    "s_cbranch_scc1 Lskip_13_%=\n"
    "s_mov_b64 exec, s[48:49]\n"
    "v_mov_b32 v112, 0xf149\n"
    "s_mov_b64 exec, -1\n"
    "Lskip_13_%=:\n"
    "s_lshl_b32 s54, s40, 1\n"
    "s_add_u32 s54, s54, 0x72000\n"
    "buffer_store_dwordx4 v[110:113], v1, s[8:11], s54 offen sc1\n"
    "Lskip_12_%=:\n"
    "s_waitcnt vmcnt(0)\n"
    "; signal 12: every wave has its write-through stores acknowledged; barrier; one wave stores the flag line\n"
    "s_barrier\n"
    "s_cmp_lg_u32 s20, 0\n"
    "s_cbranch_scc1 Lskip_14_%=\n"
    "s_add_u32 s60, s39, 0x60000\n"
    "s_add_u32 s60, s60, s38\n"
    "s_mov_b64 exec, 0xff\n"
    "buffer_store_dwordx4 v[36:39], v1, s[8:11], s60 offen sc1\n"
    "s_mov_b64 exec, -1\n"
    "Lskip_14_%=:\n"
    "ds_write_b128 v99, v[64:67] offset:2048\n"
    "ds_write_b128 v99, v[68:71] offset:2560\n"
    "v_mad_u32_u24 v103, s77, v100, v102\n"
    "v_mad_u32_u24 v103, s78, v101, v103\n"
    "v_add_u32 v104, s79, v103\n"
    "ds_read_b64_tr_b16 v[64:65], v103 offset:2048\n"
    "ds_read_b64_tr_b16 v[66:67], v104 offset:2048\n"
    "ds_read_b64_tr_b16 v[68:69], v103 offset:3072\n"
    "ds_read_b64_tr_b16 v[70:71], v104 offset:3072\n"
    "s_waitcnt lgkmcnt(2)\n"
    "buffer_store_dwordx4 v[64:67], v1, s[8:11], s75 offen sc1\n"
    "s_waitcnt lgkmcnt(0)\n"
    "buffer_store_dwordx4 v[68:71], v1, s[8:11], s76 offen sc1\n"
    "ds_write_b128 v99, v[72:75] offset:4096\n"
    "ds_write_b128 v99, v[76:79] offset:4608\n"
    "v_mad_u32_u24 v103, s86, v100, v102\n"
    "v_mad_u32_u24 v103, s87, v101, v103\n"
    "v_add_u32 v104, s88, v103\n"
    "ds_read_b64_tr_b16 v[72:73], v103 offset:4096\n"
    "ds_read_b64_tr_b16 v[74:75], v104 offset:4096\n"
    "ds_read_b64_tr_b16 v[76:77], v103 offset:5120\n"
    "ds_read_b64_tr_b16 v[78:79], v104 offset:5120\n"
    "s_waitcnt lgkmcnt(2)\n"
    "buffer_store_dwordx4 v[72:75], v1, s[8:11], s80 offen sc1\n"
    "s_waitcnt lgkmcnt(0)\n"
    "buffer_store_dwordx4 v[76:79], v1, s[8:11], s81 offen sc1\n"
    "v_permlane16_swap_b32 v80, v82\n"
    "v_permlane16_swap_b32 v81, v83\n"
    "v_permlane16_swap_b32 v84, v86\n"
    "v_permlane16_swap_b32 v85, v87\n"
    "v_lshlrev_b32 v8, 16, v80\n"
    "v_and_b32 v9, s51, v80\n"
    "v_lshlrev_b32 v10, 16, v81\n"
    "v_and_b32 v11, s51, v81\n"
    "v_lshlrev_b32 v12, 16, v82\n"
    "v_and_b32 v13, s51, v82\n"
    "v_lshlrev_b32 v14, 16, v83\n"
    "v_and_b32 v15, s51, v83\n"
    "v_lshlrev_b32 v16, 16, v84\n"
    "v_and_b32 v17, s51, v84\n"
    "v_lshlrev_b32 v18, 16, v85\n"
    "v_and_b32 v19, s51, v85\n"
    "v_lshlrev_b32 v20, 16, v86\n"
    "v_and_b32 v21, s51, v86\n"
    "v_lshlrev_b32 v22, 16, v87\n"
    "v_and_b32 v23, s51, v87\n"
    "s_and_b32 s54, s20, 1\n"
    "s_cmp_eq_u32 s54, 1\n"
    "s_mov_b32 s44, 0\n"
    "s_cselect_b32 s45, 0xffff0000, 0\n"
    "s_cselect_b32 s46, 0x10001000, 0\n"
    "s_cselect_b32 s47, 0x10001000, 0\n"
    "s_cmp_eq_u32 s20, 3\n"
    "s_cselect_b32 s56, 0x40000000, 0\n"
    "v_add_u32 v45, s56, v1\n"
    "s_cmp_eq_u32 s22, 7\n"
    "s_cselect_b32 s56, 0x40000000, 0\n"
    "v_add_u32 v41, s56, v1\n"
    "s_cmp_lg_u32 s22, 7\n"
    "s_cbranch_scc1 Lskip_15_%=\n"
    "v_mov_b32 v5, s39\n"
    "Lskip_15_%=:\n"
    "s_mul_i32 s43, s20, 0x3800\n"
    "s_mul_i32 s25, s20, 0x5000\n"
    "s_add_u32 s25, s25, 0xe000\n"
    "v_add_u32 v6, 0xe000, v1\n"
    "v_add_u32 v7, 0x18000, v1\n"
    "s_lshl_b32 s56, s20, 3\n"
    "s_add_u32 s56, s56, 0x22000\n"
    "v_lshl_add_u32 v43, v0, 5, s56\n"
    "s_mov_b32 s56, 0x22800\n"
    "v_lshl_add_u32 v49, v0, 2, s56\n"
    "v_lshl_add_u32 v50, v106, 2, s56\n"
    "v_lshl_add_u32 v51, v107, 4, s56\n"
    "s_add_u32 s75, s74, 0x240000\n"
    "s_add_u32 s76, s74, 0x480000\n"
    "s_lshr_b32 s55, s20, 1\n"
    "s_lshl_b32 s56, s22, 1\n"
    "s_add_u32 s55, s55, s56\n"
    "s_lshl_b32 s35, s55, 18\n"
    "s_add_u32 s35, s35, 0xf08000\n"
    "s_lshl_b32 s56, s54, 5\n"
    "s_add_u32 s56, s56, s55\n"
    "s_lshl_b32 s77, s56, 10\n"
    "s_lshl_b32 s36, s21, 16\n"
    "s_add_u32 s36, s36, 0x1708000\n"
    "s_mul_i32 s37, s21, 0x38000\n"
    "s_add_u32 s37, s37, 0x1b08000\n"
    "s_mul_i32 s78, s22, 3\n"
    "s_add_u32 s78, s78, s20\n"
    "s_add_u32 s78, s78, 32\n"
    "s_lshl_b32 s78, s78, 10\n"
    "s_lshl_b32 s55, s21, 1\n"
    "s_cmp_eq_u32 s54, 0\n"
    "s_cselect_b32 s56, -8, 1\n"
    "s_cselect_b32 s57, 1, -1\n"
    "s_add_i32 s55, s55, s56\n"
    "s_lshl_b32 s58, s22, 12\n"
    "s_add_u32 s58, s58, 0x72000\n"
    "s_lshl_b32 s56, s55, 12\n"
    "s_add_u32 s56, s56, s35\n"
    "s_cmp_ge_i32 s55, 0\n"
    "s_cselect_b32 s66, s56, s58\n"
    "s_cselect_b32 s79, 0x400000, 0\n"
    "s_add_i32 s55, s55, s57\n"
    "s_lshl_b32 s56, s55, 12\n"
    "s_add_u32 s56, s56, s35\n"
    "s_cmp_ge_i32 s55, 0\n"
    "s_cselect_b32 s67, s56, s58\n"
    "s_cselect_b32 s80, 0x400000, 0\n"
    "s_add_i32 s55, s55, s57\n"
    "s_lshl_b32 s56, s55, 12\n"
    "s_add_u32 s56, s56, s35\n"
    "s_cmp_ge_i32 s55, 0\n"
    "s_cselect_b32 s68, s56, s58\n"
    "s_cselect_b32 s81, 0x400000, 0\n"
    "s_add_i32 s55, s55, s57\n"
    "s_lshl_b32 s56, s55, 12\n"
    "s_add_u32 s56, s56, s35\n"
    "s_cmp_ge_i32 s55, 0\n"
    "s_cselect_b32 s69, s56, s58\n"
    "s_cselect_b32 s82, 0x400000, 0\n"
    "s_add_i32 s55, s55, s57\n"
    "s_lshl_b32 s56, s55, 12\n"
    "s_add_u32 s56, s56, s35\n"
    "s_cmp_ge_i32 s55, 0\n"
    "s_cselect_b32 s70, s56, s58\n"
    "s_cselect_b32 s83, 0x400000, 0\n"
    "s_add_i32 s55, s55, s57\n"
    "s_sleep 1\n"
    "s_sleep 1\n"
    "s_sleep 1\n"
    "s_add_u32 s71, s38, 0x60000\n"
    "buffer_load_dwordx2 v[46:47], v105, s[8:11], s71 offen sc1\n"
    "s_and_b32 s54, s20, 1\n"
    "v_and_b32 v42, 7, v0\n"
    "v_lshl_add_u32 v42, v109, 3, v42\n"
    "v_bfe_u32 v114, v0, 3, 1\n"
    "v_lshl_add_u32 v42, v114, 4, v42\n"
    "v_lshl_add_u32 v42, v115, 5, v42\n"
    "s_lshl_b32 s55, s54, 10\n"
    "v_lshl_add_u32 v42, v42, 4, s55\n"
    "v_lshl_add_u32 v44, v109, 5, v106\n"
    "v_lshl_add_u32 v44, v115, 6, v44\n"
    "s_lshl_b32 s55, s54, 8\n"
    "s_add_u32 s55, s55, 0x800\n"
    "v_lshl_add_u32 v44, v44, 4, s55\n"
    "s_lshr_b32 s55, s20, 1\n"
    "s_lshl_b32 s55, s55, 2\n"
    "s_add_u32 s55, s55, s54\n"
    "s_lshl_b32 s55, s55, 10\n"
    "s_add_u32 s55, s55, 0xe000\n"
    "v_add_u32 v52, s55, v1\n"
    "s_lshl_b32 s55, s20, 11\n"
    "s_add_u32 s55, s55, 0xe000\n"
    "v_add_u32 v53, s55, v1\n"
    "v_xor_b32 v54, 0x800, v53\n"
    "s_xor_b32 s55, s20, 0\n"
    "s_mul_i32 s55, s55, 0x1400\n"
    "s_add_u32 s55, s55, 0x10000\n"
    "v_add_u32 v55, s55, v1\n"
    "s_xor_b32 s55, s20, 1\n"
    "s_mul_i32 s55, s55, 0x1400\n"
    "s_add_u32 s55, s55, 0x10000\n"
    "v_add_u32 v254, s55, v1\n"
    "v_lshl_add_u32 v255, v115, 4, v106\n"
    "v_lshlrev_b32 v255, 4, v255\n"
    "v_lshl_add_u32 v255, v109, 14, v255\n"
    "s_lshl_b32 s55, s54, 12\n"
    "s_add_u32 s55, s55, 0x22c00\n"
    "v_add_u32 v34, s55, v1\n"
    "v_and_b32 v114, 31, v0\n"
    "v_lshlrev_b32 v116, 2, v115\n"
    "v_sub_u32 v114, v114, v116\n"
    "s_cmp_eq_u32 s54, 0\n"
    "s_cselect_b32 s56, 0, 0xf149f2ca\n"
    "s_cselect_b32 s57, 0xf149f2ca, 0\n"
    "v_mov_b32 v116, s56\n"
    "v_mov_b32 v117, s57\n"
    "v_cmp_gt_i32 s[58:59], 0, v114\n"
    "v_cmp_gt_i32 s[62:63], 1, v114\n"
    "s_nop 0\n"
    "v_cndmask_b32 v56, v117, v116, s[58:59]\n"
    "v_cmp_gt_i32 s[58:59], 2, v114\n"
    "v_cndmask_b32 v57, v117, v116, s[62:63]\n"
    "v_cmp_gt_i32 s[62:63], 3, v114\n"
    "v_cndmask_b32 v58, v117, v116, s[58:59]\n"
    "v_cmp_gt_i32 s[58:59], 16, v114\n"
    "v_cndmask_b32 v59, v117, v116, s[62:63]\n"
    "v_cmp_gt_i32 s[62:63], 17, v114\n"
    "v_cndmask_b32 v60, v117, v116, s[58:59]\n"
    "v_cmp_gt_i32 s[58:59], 18, v114\n"
    "v_cndmask_b32 v61, v117, v116, s[62:63]\n"
    "v_cmp_gt_i32 s[62:63], 19, v114\n"
    "v_cndmask_b32 v62, v117, v116, s[58:59]\n"
    "v_cmp_gt_i32 s[58:59], 8, v114\n"
    "v_cndmask_b32 v63, v117, v116, s[62:63]\n"
    "v_cmp_gt_i32 s[62:63], 9, v114\n"
    "v_cndmask_b32 v64, v117, v116, s[58:59]\n"
    "v_cmp_gt_i32 s[58:59], 10, v114\n"
    "v_cndmask_b32 v65, v117, v116, s[62:63]\n"
    "v_cmp_gt_i32 s[62:63], 11, v114\n"
    "v_cndmask_b32 v66, v117, v116, s[58:59]\n"
    "v_cmp_gt_i32 s[58:59], 24, v114\n"
    "v_cndmask_b32 v67, v117, v116, s[62:63]\n"
    "v_cmp_gt_i32 s[62:63], 25, v114\n"
    "v_cndmask_b32 v68, v117, v116, s[58:59]\n"
    "v_cmp_gt_i32 s[58:59], 26, v114\n"
    "v_cndmask_b32 v69, v117, v116, s[62:63]\n"
    "v_cmp_gt_i32 s[62:63], 27, v114\n"
    "v_cndmask_b32 v70, v117, v116, s[58:59]\n"
    "s_nop 0\n"
    "v_cndmask_b32 v71, v117, v116, s[62:63]\n"
    "ds_write_b128 v34, v[56:59] offset:0\n"
    "ds_write_b128 v34, v[60:63] offset:1024\n"
    "ds_write_b128 v34, v[64:67] offset:2048\n"
    "ds_write_b128 v34, v[68:71] offset:3072\n"
    "; signal 13: every wave has its write-through stores acknowledged; barrier; one wave stores the flag line\n"
    "s_waitcnt vmcnt(1)\n"
    "s_barrier\n"
    "s_cmp_lg_u32 s20, 0\n"
    "s_cbranch_scc1 Lskip_16_%=\n"
    "s_add_u32 s60, s39, 0x68000\n"
    "s_add_u32 s60, s60, s38\n"
    "s_mov_b64 exec, 0xff\n"
    "buffer_store_dwordx4 v[36:39], v1, s[8:11], s60 offen sc1\n"
    "s_mov_b64 exec, -1\n"
    "Lskip_16_%=:\n"
    "s_add_u32 s65, s74, s43\n"
    "s_lshr_b32 s73, s43, 2\n"
    "s_add_u32 s73, s73, s74\n"
    "s_add_u32 s73, s73, 0xe000\n"
    "s_waitcnt vmcnt(0)\n"
    "v_cmp_ne_u64 vcc, s[26:27], v[46:47]\n"
    "s_nop 0\n"
    "s_cbranch_vccnz Lspin_17_%=\n"
    "Lgo_18_%=:\n"
    "; K and V projections\n"
    "v_mov_b32 v198, s42\n"
    "v_mov_b32 v199, s42\n"
    "v_mov_b32 v200, s42\n"
    "v_mov_b32 v201, s42\n"
    "s_add_u32 s53, s65, 0x0\n"
    "s_add_u32 m0, s43, 0x0\n"
    "s_nop 0\n"
    "buffer_load_dwordx4 v1, s[8:11], s53 offen offset:0 sc1 lds\n"
    "buffer_load_dwordx4 v1, s[8:11], s53 offen offset:1024 sc1 lds\n"
    "buffer_load_dwordx4 a[0:3], v1, s[8:11], s28 offen offset:0 sc1\n"
    "buffer_load_dwordx4 a[4:7], v1, s[8:11], s28 offen offset:1024 sc1\n"
    "buffer_load_dwordx4 v1, s[8:11], s53 offen offset:2048 sc1 lds\n"
    "buffer_load_dwordx4 a[8:11], v1, s[8:11], s28 offen offset:2048 sc1\n"
    "buffer_load_dwordx4 a[12:15], v1, s[8:11], s28 offen offset:3072 sc1\n"
    "buffer_load_dwordx4 v1, s[8:11], s53 offen offset:3072 sc1 lds\n"
    "s_add_u32 s52, s28, 0x1000\n"
    "buffer_load_dwordx4 a[16:19], v1, s[8:11], s52 offen offset:0 sc1\n"
    "buffer_load_dwordx4 a[20:23], v1, s[8:11], s52 offen offset:1024 sc1\n"
    "s_waitcnt vmcnt(8)\n"
    "s_barrier\n"
    "ds_read_b128 v[56:59], v1 offset:0\n"
    "ds_read_b128 v[60:63], v1 offset:14336\n"
    "ds_read_b128 v[64:67], v1 offset:28672\n"
    "ds_read_b128 v[68:71], v1 offset:43008\n"
    "s_add_u32 s53, s65, 0x1000\n"
    "s_add_u32 m0, s43, 0x1000\n"
    "s_nop 0\n"
    "buffer_load_dwordx4 v1, s[8:11], s53 offen offset:0 sc1 lds\n"
    "buffer_load_dwordx4 a[24:27], v1, s[8:11], s52 offen offset:2048 sc1\n"
    "s_waitcnt vmcnt(9)\n"
    "v_mfma_f32_16x16x32_bf16 v[24:27], a[0:3], v[198:201], 0\n"
    "s_waitcnt vmcnt(8)\n"
    "v_mfma_f32_16x16x32_bf16 v[202:205], v[198:201], a[4:7], 0\n"
    "s_waitcnt lgkmcnt(3)\n"
    "v_mfma_f32_16x16x32_bf16 v[118:121], a[0:3], v[56:59], 0\n"
    "ds_read_b128 v[72:75], v1 offset:1024\n"
    "v_mfma_f32_16x16x32_bf16 v[134:137], v[56:59], a[4:7], 0\n"
    "buffer_load_dwordx4 a[28:31], v1, s[8:11], s52 offen offset:3072 sc1\n"
    "s_waitcnt lgkmcnt(3)\n"
    "v_mfma_f32_16x16x32_bf16 v[122:125], a[0:3], v[60:63], 0\n"
    "ds_read_b128 v[76:79], v1 offset:15360\n"
    "v_mfma_f32_16x16x32_bf16 v[138:141], v[60:63], a[4:7], 0\n"
    "buffer_load_dwordx4 v1, s[8:11], s53 offen offset:1024 sc1 lds\n"
    "s_waitcnt lgkmcnt(3)\n"
    "v_mfma_f32_16x16x32_bf16 v[126:129], a[0:3], v[64:67], 0\n"
    "ds_read_b128 v[80:83], v1 offset:29696\n"
    "v_mfma_f32_16x16x32_bf16 v[142:145], v[64:67], a[4:7], 0\n"
    "s_waitcnt lgkmcnt(3)\n"
    "v_mfma_f32_16x16x32_bf16 v[130:133], a[0:3], v[68:71], 0\n"
    "ds_read_b128 v[84:87], v1 offset:44032\n"
    "v_mfma_f32_16x16x32_bf16 v[146:149], v[68:71], a[4:7], 0\n"
    "s_add_u32 s52, s28, 0x2000\n"
    "buffer_load_dwordx4 a[32:35], v1, s[8:11], s52 offen offset:0 sc1\n"
    "buffer_load_dwordx4 a[36:39], v1, s[8:11], s52 offen offset:1024 sc1\n"
    "s_waitcnt vmcnt(8)\n"
    "s_barrier\n"
    "v_mfma_f32_16x16x32_bf16 v[24:27], a[8:11], v[198:201], v[24:27]\n"
    "v_mfma_f32_16x16x32_bf16 v[202:205], v[198:201], a[12:15], v[202:205]\n"
    "s_waitcnt lgkmcnt(3)\n"
    "v_mfma_f32_16x16x32_bf16 v[118:121], a[8:11], v[72:75], v[118:121]\n"
    "ds_read_b128 v[56:59], v1 offset:2048\n"
    "v_mfma_f32_16x16x32_bf16 v[134:137], v[72:75], a[12:15], v[134:137]\n"
    "buffer_load_dwordx2 v[88:89], v3, s[8:11], s73 offen offset:0 sc1\n"
    "s_waitcnt lgkmcnt(3)\n"
    "v_mfma_f32_16x16x32_bf16 v[122:125], a[8:11], v[76:79], v[122:125]\n"
    "ds_read_b128 v[60:63], v1 offset:16384\n"
    "v_mfma_f32_16x16x32_bf16 v[138:141], v[76:79], a[12:15], v[138:141]\n"
    "buffer_load_dwordx2 v[90:91], v3, s[8:11], s73 offen offset:512 sc1\n"
    "s_waitcnt lgkmcnt(3)\n"
    "v_mfma_f32_16x16x32_bf16 v[126:129], a[8:11], v[80:83], v[126:129]\n"
    "ds_read_b128 v[64:67], v1 offset:30720\n"
    "v_mfma_f32_16x16x32_bf16 v[142:145], v[80:83], a[12:15], v[142:145]\n"
    "buffer_load_dwordx2 v[92:93], v3, s[8:11], s73 offen offset:1024 sc1\n"
    "s_waitcnt lgkmcnt(3)\n"
    "v_mfma_f32_16x16x32_bf16 v[130:133], a[8:11], v[84:87], v[130:133]\n"
    "ds_read_b128 v[68:71], v1 offset:45056\n"
    "v_mfma_f32_16x16x32_bf16 v[146:149], v[84:87], a[12:15], v[146:149]\n"
    "buffer_load_dwordx2 v[94:95], v3, s[8:11], s73 offen offset:1536 sc1\n"
    "s_waitcnt vmcnt(11)\n"
    "v_mfma_f32_16x16x32_bf16 v[24:27], a[16:19], v[198:201], v[24:27]\n"
    "s_waitcnt vmcnt(10)\n"
    "v_mfma_f32_16x16x32_bf16 v[202:205], v[198:201], a[20:23], v[202:205]\n"
    "s_waitcnt lgkmcnt(3)\n"
    "v_mfma_f32_16x16x32_bf16 v[118:121], a[16:19], v[56:59], v[118:121]\n"
    "ds_read_b128 v[72:75], v1 offset:3072\n"
    "v_mfma_f32_16x16x32_bf16 v[134:137], v[56:59], a[20:23], v[134:137]\n"
    "buffer_load_dwordx2 v[96:97], v3, s[8:11], s73 offen offset:2048 sc1\n"
    "s_waitcnt lgkmcnt(3)\n"
    "v_mfma_f32_16x16x32_bf16 v[122:125], a[16:19], v[60:63], v[122:125]\n"
    "ds_read_b128 v[76:79], v1 offset:17408\n"
    "v_mfma_f32_16x16x32_bf16 v[138:141], v[60:63], a[20:23], v[138:141]\n"
    "buffer_load_dwordx2 v[98:99], v3, s[8:11], s73 offen offset:2560 sc1\n"
    "s_waitcnt lgkmcnt(3)\n"
    "v_mfma_f32_16x16x32_bf16 v[126:129], a[16:19], v[64:67], v[126:129]\n"
    "ds_read_b128 v[80:83], v1 offset:31744\n"
    "v_mfma_f32_16x16x32_bf16 v[142:145], v[64:67], a[20:23], v[142:145]\n"
    "buffer_load_dwordx2 v[100:101], v3, s[8:11], s73 offen offset:3072 sc1\n"
    "s_waitcnt lgkmcnt(3)\n"
    "v_mfma_f32_16x16x32_bf16 v[130:133], a[16:19], v[68:71], v[130:133]\n"
    "ds_read_b128 v[84:87], v1 offset:46080\n"
    "v_mfma_f32_16x16x32_bf16 v[146:149], v[68:71], a[20:23], v[146:149]\n"
    "buffer_load_dwordx4 v1, s[8:11], s53 offen offset:2048 sc1 lds\n"
    "buffer_load_dwordx4 a[40:43], v1, s[8:11], s52 offen offset:2048 sc1\n"
    "buffer_load_dwordx4 a[44:47], v1, s[8:11], s52 offen offset:3072 sc1\n"
    "buffer_load_dwordx4 v1, s[8:11], s53 offen offset:3072 sc1 lds\n"
    "s_add_u32 s52, s28, 0x3000\n"
    "buffer_load_dwordx4 a[48:51], v1, s[8:11], s52 offen offset:0 sc1\n"
    "buffer_load_dwordx4 a[52:55], v1, s[8:11], s52 offen offset:1024 sc1\n"
    "s_add_u32 s53, s65, 0x2000\n"
    "s_add_u32 m0, s43, 0x2000\n"
    "s_nop 0\n"
    "buffer_load_dwordx4 v1, s[8:11], s53 offen offset:0 sc1 lds\n"
    "buffer_load_dwordx4 a[56:59], v1, s[8:11], s52 offen offset:2048 sc1\n"
    "buffer_load_dwordx4 a[60:63], v1, s[8:11], s52 offen offset:3072 sc1\n"
    "s_waitcnt vmcnt(8)\n"
    "s_barrier\n"
    "v_mfma_f32_16x16x32_bf16 v[24:27], a[24:27], v[198:201], v[24:27]\n"
    "v_mfma_f32_16x16x32_bf16 v[202:205], v[198:201], a[28:31], v[202:205]\n"
    "s_waitcnt lgkmcnt(3)\n"
    "v_mfma_f32_16x16x32_bf16 v[118:121], a[24:27], v[72:75], v[118:121]\n"
    "ds_read_b128 v[56:59], v1 offset:4096\n"
    "v_mfma_f32_16x16x32_bf16 v[134:137], v[72:75], a[28:31], v[134:137]\n"
    "buffer_load_dwordx4 v1, s[8:11], s53 offen offset:1024 sc1 lds\n"
    "s_waitcnt lgkmcnt(3)\n"
    "v_mfma_f32_16x16x32_bf16 v[122:125], a[24:27], v[76:79], v[122:125]\n"
    "ds_read_b128 v[60:63], v1 offset:18432\n"
    "v_mfma_f32_16x16x32_bf16 v[138:141], v[76:79], a[28:31], v[138:141]\n"
    "s_add_u32 s52, s28, 0x4000\n"
    "buffer_load_dwordx4 a[64:67], v1, s[8:11], s52 offen offset:0 sc1\n"
    "s_waitcnt lgkmcnt(3)\n"
    "v_mfma_f32_16x16x32_bf16 v[126:129], a[24:27], v[80:83], v[126:129]\n"
    "ds_read_b128 v[64:67], v1 offset:32768\n"
    "v_mfma_f32_16x16x32_bf16 v[142:145], v[80:83], a[28:31], v[142:145]\n"
    "buffer_load_dwordx4 a[68:71], v1, s[8:11], s52 offen offset:1024 sc1\n"
    "s_waitcnt lgkmcnt(3)\n"
    "v_mfma_f32_16x16x32_bf16 v[130:133], a[24:27], v[84:87], v[130:133]\n"
    "ds_read_b128 v[68:71], v1 offset:47104\n"
    "v_mfma_f32_16x16x32_bf16 v[146:149], v[84:87], a[28:31], v[146:149]\n"
    "buffer_load_dwordx4 v1, s[8:11], s53 offen offset:2048 sc1 lds\n"
    "v_mfma_f32_16x16x32_bf16 v[24:27], a[32:35], v[198:201], v[24:27]\n"
    "v_mfma_f32_16x16x32_bf16 v[202:205], v[198:201], a[36:39], v[202:205]\n"
    "s_waitcnt lgkmcnt(3)\n"
    "v_mfma_f32_16x16x32_bf16 v[118:121], a[32:35], v[56:59], v[118:121]\n"
    "ds_read_b128 v[72:75], v1 offset:5120\n"
    "v_mfma_f32_16x16x32_bf16 v[134:137], v[56:59], a[36:39], v[134:137]\n"
    "buffer_load_dwordx4 a[72:75], v1, s[8:11], s52 offen offset:2048 sc1\n"
    "s_waitcnt lgkmcnt(3)\n"
    "v_mfma_f32_16x16x32_bf16 v[122:125], a[32:35], v[60:63], v[122:125]\n"
    "ds_read_b128 v[76:79], v1 offset:19456\n"
    "v_mfma_f32_16x16x32_bf16 v[138:141], v[60:63], a[36:39], v[138:141]\n"
    "s_waitcnt lgkmcnt(3)\n"
    "v_mfma_f32_16x16x32_bf16 v[126:129], a[32:35], v[64:67], v[126:129]\n"
    "ds_read_b128 v[80:83], v1 offset:33792\n"
    "v_mfma_f32_16x16x32_bf16 v[142:145], v[64:67], a[36:39], v[142:145]\n"
    "buffer_load_dwordx4 a[76:79], v1, s[8:11], s52 offen offset:3072 sc1\n"
    "s_waitcnt lgkmcnt(3)\n"
    "v_mfma_f32_16x16x32_bf16 v[130:133], a[32:35], v[68:71], v[130:133]\n"
    "ds_read_b128 v[84:87], v1 offset:48128\n"
    "v_mfma_f32_16x16x32_bf16 v[146:149], v[68:71], a[36:39], v[146:149]\n"
    "s_waitcnt vmcnt(13)\n"
    "v_mfma_f32_16x16x32_bf16 v[24:27], a[40:43], v[198:201], v[24:27]\n"
    "s_waitcnt vmcnt(12)\n"
    "v_mfma_f32_16x16x32_bf16 v[202:205], v[198:201], a[44:47], v[202:205]\n"
    "s_waitcnt lgkmcnt(3)\n"
    "v_mfma_f32_16x16x32_bf16 v[118:121], a[40:43], v[72:75], v[118:121]\n"
    "ds_read_b128 v[56:59], v1 offset:6144\n"
    "v_mfma_f32_16x16x32_bf16 v[134:137], v[72:75], a[44:47], v[134:137]\n"
    "buffer_load_dwordx4 v1, s[8:11], s53 offen offset:3072 sc1 lds\n"
    "s_waitcnt lgkmcnt(3)\n"
    "v_mfma_f32_16x16x32_bf16 v[122:125], a[40:43], v[76:79], v[122:125]\n"
    "ds_read_b128 v[60:63], v1 offset:20480\n"
    "v_mfma_f32_16x16x32_bf16 v[138:141], v[76:79], a[44:47], v[138:141]\n"
    "s_add_u32 s52, s28, 0x5000\n"
    "buffer_load_dwordx4 a[80:83], v1, s[8:11], s52 offen offset:0 sc1\n"
    "s_waitcnt lgkmcnt(3)\n"
    "v_mfma_f32_16x16x32_bf16 v[126:129], a[40:43], v[80:83], v[126:129]\n"
    "ds_read_b128 v[64:67], v1 offset:34816\n"
    "v_mfma_f32_16x16x32_bf16 v[142:145], v[80:83], a[44:47], v[142:145]\n"
    "s_waitcnt lgkmcnt(3)\n"
    "v_mfma_f32_16x16x32_bf16 v[130:133], a[40:43], v[84:87], v[130:133]\n"
    "ds_read_b128 v[68:71], v1 offset:49152\n"
    "v_mfma_f32_16x16x32_bf16 v[146:149], v[84:87], a[44:47], v[146:149]\n"
    "buffer_load_dwordx4 a[84:87], v1, s[8:11], s52 offen offset:1024 sc1\n"
    "s_waitcnt vmcnt(11)\n"
    "s_barrier\n"
    "v_mfma_f32_16x16x32_bf16 v[24:27], a[48:51], v[198:201], v[24:27]\n"
    "v_mfma_f32_16x16x32_bf16 v[202:205], v[198:201], a[52:55], v[202:205]\n"
    "s_waitcnt lgkmcnt(3)\n"
    "v_mfma_f32_16x16x32_bf16 v[118:121], a[48:51], v[56:59], v[118:121]\n"
    "ds_read_b128 v[72:75], v1 offset:7168\n"
    "v_mfma_f32_16x16x32_bf16 v[134:137], v[56:59], a[52:55], v[134:137]\n"
    "v_add_f32 v88, v88, v90\n"
    "s_add_u32 s53, s65, 0x3000\n"
    "s_add_u32 m0, s43, 0x3000\n"
    "s_nop 0\n"
    "buffer_load_dwordx4 v1, s[8:11], s53 offen offset:0 sc1 lds\n"
    "s_waitcnt lgkmcnt(3)\n"
    "v_mfma_f32_16x16x32_bf16 v[122:125], a[48:51], v[60:63], v[122:125]\n"
    "ds_read_b128 v[76:79], v1 offset:21504\n"
    "v_add_f32 v89, v89, v91\n"
    "v_mfma_f32_16x16x32_bf16 v[138:141], v[60:63], a[52:55], v[138:141]\n"
    "v_add_f32 v92, v92, v94\n"
    "s_waitcnt lgkmcnt(3)\n"
    "v_mfma_f32_16x16x32_bf16 v[126:129], a[48:51], v[64:67], v[126:129]\n"
    "ds_read_b128 v[80:83], v1 offset:35840\n"
    "v_add_f32 v93, v93, v95\n"
    "v_mfma_f32_16x16x32_bf16 v[142:145], v[64:67], a[52:55], v[142:145]\n"
    "v_add_f32 v96, v96, v98\n"
    "buffer_load_dwordx4 a[88:91], v1, s[8:11], s52 offen offset:2048 sc1\n"
    "s_waitcnt lgkmcnt(3)\n"
    "v_mfma_f32_16x16x32_bf16 v[130:133], a[48:51], v[68:71], v[130:133]\n"
    "ds_read_b128 v[84:87], v1 offset:50176\n"
    "v_add_f32 v97, v97, v99\n"
    "v_mfma_f32_16x16x32_bf16 v[146:149], v[68:71], a[52:55], v[146:149]\n"
    "v_add_f32 v88, v88, v92\n"
    "s_waitcnt vmcnt(12)\n"
    "v_mfma_f32_16x16x32_bf16 v[24:27], a[56:59], v[198:201], v[24:27]\n"
    "s_waitcnt vmcnt(11)\n"
    "v_mfma_f32_16x16x32_bf16 v[202:205], v[198:201], a[60:63], v[202:205]\n"
    "s_waitcnt lgkmcnt(3)\n"
    "v_mfma_f32_16x16x32_bf16 v[118:121], a[56:59], v[72:75], v[118:121]\n"
    "ds_read_b128 v[56:59], v1 offset:8192\n"
    "v_add_f32 v89, v89, v93\n"
    "v_mfma_f32_16x16x32_bf16 v[134:137], v[72:75], a[60:63], v[134:137]\n"
    "v_add_f32 v96, v96, v100\n"
    "buffer_load_dwordx4 a[92:95], v1, s[8:11], s52 offen offset:3072 sc1\n"
    "s_waitcnt lgkmcnt(3)\n"
    "v_mfma_f32_16x16x32_bf16 v[122:125], a[56:59], v[76:79], v[122:125]\n"
    "ds_read_b128 v[60:63], v1 offset:22528\n"
    "v_add_f32 v97, v97, v101\n"
    "v_mfma_f32_16x16x32_bf16 v[138:141], v[76:79], a[60:63], v[138:141]\n"
    "v_add_f32 v88, v88, v96\n"
    "buffer_load_dwordx4 v1, s[8:11], s53 offen offset:1024 sc1 lds\n"
    "s_waitcnt lgkmcnt(3)\n"
    "v_mfma_f32_16x16x32_bf16 v[126:129], a[56:59], v[80:83], v[126:129]\n"
    "ds_read_b128 v[64:67], v1 offset:36864\n"
    "v_add_f32 v89, v89, v97\n"
    "v_mfma_f32_16x16x32_bf16 v[142:145], v[80:83], a[60:63], v[142:145]\n"
    "ds_write_b64 v43, v[88:89]\n"
    "s_add_u32 s52, s28, 0x6000\n"
    "buffer_load_dwordx4 a[96:99], v1, s[8:11], s52 offen offset:0 sc1\n"
    "s_waitcnt lgkmcnt(4)\n"
    "v_mfma_f32_16x16x32_bf16 v[130:133], a[56:59], v[84:87], v[130:133]\n"
    "ds_read_b128 v[68:71], v1 offset:51200\n"
    "v_mfma_f32_16x16x32_bf16 v[146:149], v[84:87], a[60:63], v[146:149]\n"
    "buffer_load_dwordx4 a[100:103], v1, s[8:11], s52 offen offset:1024 sc1\n"
    "buffer_load_dwordx4 a[104:107], v1, s[8:11], s52 offen offset:2048 sc1\n"
    "buffer_load_dwordx4 a[108:111], v1, s[8:11], s52 offen offset:3072 sc1\n"
    "s_add_u32 s52, s28, 0x7000\n"
    "buffer_load_dwordx4 a[112:115], v1, s[8:11], s52 offen offset:0 sc1\n"
    "buffer_load_dwordx4 a[116:119], v1, s[8:11], s52 offen offset:1024 sc1\n"
    "buffer_load_dwordx4 a[120:123], v1, s[8:11], s52 offen offset:2048 sc1\n"
    "buffer_load_dwordx4 a[124:127], v1, s[8:11], s52 offen offset:3072 sc1\n"
    "s_waitcnt vmcnt(8)\n"
    "s_waitcnt lgkmcnt(1)\n"
    "s_barrier\n"
    "v_mfma_f32_16x16x32_bf16 v[24:27], a[64:67], v[198:201], v[24:27]\n"
    "v_mfma_f32_16x16x32_bf16 v[202:205], v[198:201], a[68:71], v[202:205]\n"
    "v_mfma_f32_16x16x32_bf16 v[118:121], a[64:67], v[56:59], v[118:121]\n"
    "ds_read_b128 v[72:75], v1 offset:9216\n"
    "v_and_b32 v88, 0xffffffe0, v43\n"
    "v_mfma_f32_16x16x32_bf16 v[134:137], v[56:59], a[68:71], v[134:137]\n"
    "ds_read_b128 v[102:105], v88\n"
    "s_add_u32 s52, s28, 0x8000\n"
    "buffer_load_dwordx4 a[128:131], v1, s[8:11], s52 offen offset:0 sc1\n"
    "v_mfma_f32_16x16x32_bf16 v[122:125], a[64:67], v[60:63], v[122:125]\n"
    "ds_read_b128 v[76:79], v1 offset:23552\n"
    "ds_read_b128 v[106:109], v88 offset:16\n"
    "v_mfma_f32_16x16x32_bf16 v[138:141], v[60:63], a[68:71], v[138:141]\n"
    "buffer_load_dwordx4 a[132:135], v1, s[8:11], s52 offen offset:1024 sc1\n"
    "v_mfma_f32_16x16x32_bf16 v[126:129], a[64:67], v[64:67], v[126:129]\n"
    "ds_read_b128 v[80:83], v1 offset:37888\n"
    "v_mfma_f32_16x16x32_bf16 v[142:145], v[64:67], a[68:71], v[142:145]\n"
    "buffer_load_dwordx4 a[136:139], v1, s[8:11], s52 offen offset:2048 sc1\n"
    "s_waitcnt lgkmcnt(5)\n"
    "v_mfma_f32_16x16x32_bf16 v[130:133], a[64:67], v[68:71], v[130:133]\n"
    "ds_read_b128 v[84:87], v1 offset:52224\n"
    "v_mfma_f32_16x16x32_bf16 v[146:149], v[68:71], a[68:71], v[146:149]\n"
    "buffer_load_dwordx4 a[140:143], v1, s[8:11], s52 offen offset:3072 sc1\n"
    "v_mfma_f32_16x16x32_bf16 v[24:27], a[72:75], v[198:201], v[24:27]\n"
    "v_mfma_f32_16x16x32_bf16 v[202:205], v[198:201], a[76:79], v[202:205]\n"
    "s_waitcnt lgkmcnt(5)\n"
    "v_mfma_f32_16x16x32_bf16 v[118:121], a[72:75], v[72:75], v[118:121]\n"
    "ds_read_b128 v[56:59], v1 offset:10240\n"
    "s_waitcnt lgkmcnt(5)\n"
    "v_add_f32 v102, v102, v104\n"
    "v_mfma_f32_16x16x32_bf16 v[134:137], v[72:75], a[76:79], v[134:137]\n"
    "v_add_f32 v103, v103, v105\n"
    "s_add_u32 s52, s28, 0x9000\n"
    "buffer_load_dwordx4 a[144:147], v1, s[8:11], s52 offen offset:0 sc1\n"
    "s_waitcnt lgkmcnt(4)\n"
    "v_mfma_f32_16x16x32_bf16 v[122:125], a[72:75], v[76:79], v[122:125]\n"
    "ds_read_b128 v[60:63], v1 offset:24576\n"
    "s_waitcnt lgkmcnt(4)\n"
    "v_add_f32 v106, v106, v108\n"
    "v_mfma_f32_16x16x32_bf16 v[138:141], v[76:79], a[76:79], v[138:141]\n"
    "v_add_f32 v107, v107, v109\n"
    "s_waitcnt lgkmcnt(3)\n"
    "v_mfma_f32_16x16x32_bf16 v[126:129], a[72:75], v[80:83], v[126:129]\n"
    "ds_read_b128 v[64:67], v1 offset:38912\n"
    "v_add_f32 v102, v102, v106\n"
    "v_mfma_f32_16x16x32_bf16 v[142:145], v[80:83], a[76:79], v[142:145]\n"
    "v_add_f32 v103, v103, v107\n"
    "buffer_load_dwordx4 a[148:151], v1, s[8:11], s52 offen offset:1024 sc1\n"
    "s_waitcnt lgkmcnt(3)\n"
    "v_mfma_f32_16x16x32_bf16 v[130:133], a[72:75], v[84:87], v[130:133]\n"
    "ds_read_b128 v[68:71], v1 offset:53248\n"
    "v_mul_f32 v102, 0x3b124925, v102\n"
    "v_mfma_f32_16x16x32_bf16 v[146:149], v[84:87], a[76:79], v[146:149]\n"
    "v_mul_f32 v103, 0x3b124925, v103\n"
    "buffer_load_dwordx4 a[152:155], v1, s[8:11], s52 offen offset:2048 sc1\n"
    "v_mfma_f32_16x16x32_bf16 v[24:27], a[80:83], v[198:201], v[24:27]\n"
    "v_mfma_f32_16x16x32_bf16 v[202:205], v[198:201], a[84:87], v[202:205]\n"
    "s_waitcnt lgkmcnt(3)\n"
    "v_mfma_f32_16x16x32_bf16 v[118:121], a[80:83], v[56:59], v[118:121]\n"
    "ds_read_b128 v[72:75], v1 offset:11264\n"
    "v_fma_f32 v103, -v102, v102, v103\n"
    "v_mfma_f32_16x16x32_bf16 v[134:137], v[56:59], a[84:87], v[134:137]\n"
    "v_max_f32 v103, 0, v103\n"
    "buffer_load_dwordx4 a[156:159], v1, s[8:11], s52 offen offset:3072 sc1\n"
    "s_waitcnt lgkmcnt(3)\n"
    "v_mfma_f32_16x16x32_bf16 v[122:125], a[80:83], v[60:63], v[122:125]\n"
    "ds_read_b128 v[76:79], v1 offset:25600\n"
    "v_add_f32 v103, 0x3727c5ac, v103\n"
    "v_mfma_f32_16x16x32_bf16 v[138:141], v[60:63], a[84:87], v[138:141]\n"
    "v_rsq_f32 v103, v103\n"
    "s_waitcnt lgkmcnt(3)\n"
    "v_mfma_f32_16x16x32_bf16 v[126:129], a[80:83], v[64:67], v[126:129]\n"
    "ds_read_b128 v[80:83], v1 offset:39936\n"
    "v_mfma_f32_16x16x32_bf16 v[142:145], v[64:67], a[84:87], v[142:145]\n"
    "ds_write2st64_b32 v49, v102, v103 offset1:1\n"
    "s_add_u32 s52, s28, 0xa000\n"
    "buffer_load_dwordx4 a[160:163], v1, s[8:11], s52 offen offset:0 sc1\n"
    "s_waitcnt lgkmcnt(4)\n"
    "v_mfma_f32_16x16x32_bf16 v[130:133], a[80:83], v[68:71], v[130:133]\n"
    "ds_read_b128 v[84:87], v1 offset:54272\n"
    "ds_read2_b32 v[110:111], v50 offset0:0 offset1:64\n"
    "v_mfma_f32_16x16x32_bf16 v[146:149], v[68:71], a[84:87], v[146:149]\n"
    "ds_read2_b32 v[112:113], v50 offset0:16 offset1:80\n"
    "v_mfma_f32_16x16x32_bf16 v[24:27], a[88:91], v[198:201], v[24:27]\n"
    "v_mfma_f32_16x16x32_bf16 v[202:205], v[198:201], a[92:95], v[202:205]\n"
    "s_waitcnt lgkmcnt(6)\n"
    "v_mfma_f32_16x16x32_bf16 v[118:121], a[88:91], v[72:75], v[118:121]\n"
    "ds_read_b128 v[56:59], v1 offset:12288\n"
    "ds_read2_b32 v[114:115], v50 offset0:32 offset1:96\n"
    "v_mfma_f32_16x16x32_bf16 v[134:137], v[72:75], a[92:95], v[134:137]\n"
    "ds_read2_b32 v[116:117], v50 offset0:48 offset1:112\n"
    "s_waitcnt lgkmcnt(8)\n"
    "v_mfma_f32_16x16x32_bf16 v[122:125], a[88:91], v[76:79], v[122:125]\n"
    "ds_read_b128 v[60:63], v1 offset:26624\n"
    "ds_read_b128 v[166:169], v51 offset:0\n"
    "v_mfma_f32_16x16x32_bf16 v[138:141], v[76:79], a[92:95], v[138:141]\n"
    "ds_read_b128 v[170:173], v51 offset:256\n"
    "buffer_load_dwordx4 a[164:167], v1, s[8:11], s52 offen offset:1024 sc1\n"
    "s_waitcnt lgkmcnt(10)\n"
    "v_mfma_f32_16x16x32_bf16 v[126:129], a[88:91], v[80:83], v[126:129]\n"
    "ds_read_b128 v[64:67], v1 offset:40960\n"
    "ds_read_b128 v[174:177], v51 offset:64\n"
    "v_mfma_f32_16x16x32_bf16 v[142:145], v[80:83], a[92:95], v[142:145]\n"
    "ds_read_b128 v[178:181], v51 offset:320\n"
    "s_waitcnt lgkmcnt(11)\n"
    "v_mfma_f32_16x16x32_bf16 v[130:133], a[88:91], v[84:87], v[130:133]\n"
    "ds_read_b128 v[68:71], v1 offset:55296\n"
    "ds_read_b128 v[182:185], v51 offset:128\n"
    "v_mfma_f32_16x16x32_bf16 v[146:149], v[84:87], a[92:95], v[146:149]\n"
    "ds_read_b128 v[186:189], v51 offset:384\n"
    "s_waitcnt vmcnt(17)\n"
    "v_mfma_f32_16x16x32_bf16 v[24:27], a[96:99], v[198:201], v[24:27]\n"
    "s_waitcnt vmcnt(16)\n"
    "v_mfma_f32_16x16x32_bf16 v[202:205], v[198:201], a[100:103], v[202:205]\n"
    "s_waitcnt lgkmcnt(11)\n"
    "v_mfma_f32_16x16x32_bf16 v[118:121], a[96:99], v[56:59], v[118:121]\n"
    "ds_read_b128 v[72:75], v1 offset:13312\n"
    "ds_read_b128 v[190:193], v51 offset:192\n"
    "v_mfma_f32_16x16x32_bf16 v[134:137], v[56:59], a[100:103], v[134:137]\n"
    "ds_read_b128 v[194:197], v51 offset:448\n"
    "s_waitcnt lgkmcnt(11)\n"
    "v_mfma_f32_16x16x32_bf16 v[122:125], a[96:99], v[60:63], v[122:125]\n"
    "ds_read_b128 v[76:79], v1 offset:27648\n"
    "v_mfma_f32_16x16x32_bf16 v[138:141], v[60:63], a[100:103], v[138:141]\n"
    "s_waitcnt lgkmcnt(9)\n"
    "v_mfma_f32_16x16x32_bf16 v[126:129], a[96:99], v[64:67], v[126:129]\n"
    "ds_read_b128 v[80:83], v1 offset:41984\n"
    "v_mfma_f32_16x16x32_bf16 v[142:145], v[64:67], a[100:103], v[142:145]\n"
    "s_waitcnt lgkmcnt(7)\n"
    "v_mfma_f32_16x16x32_bf16 v[130:133], a[96:99], v[68:71], v[130:133]\n"
    "ds_read_b128 v[84:87], v1 offset:56320\n"
    "v_mfma_f32_16x16x32_bf16 v[146:149], v[68:71], a[100:103], v[146:149]\n"
    "s_waitcnt vmcnt(15)\n"
    "v_mfma_f32_16x16x32_bf16 v[24:27], a[104:107], v[198:201], v[24:27]\n"
    "s_waitcnt vmcnt(14)\n"
    "v_mfma_f32_16x16x32_bf16 v[202:205], v[198:201], a[108:111], v[202:205]\n"
    "s_waitcnt lgkmcnt(5)\n"
    "v_mfma_f32_16x16x32_bf16 v[118:121], a[104:107], v[72:75], v[118:121]\n"
    "ds_read_b128 v[56:59], v1 offset:0\n"
    "v_mfma_f32_16x16x32_bf16 v[134:137], v[72:75], a[108:111], v[134:137]\n"
    "s_waitcnt lgkmcnt(3)\n"
    "v_mfma_f32_16x16x32_bf16 v[122:125], a[104:107], v[76:79], v[122:125]\n"
    "ds_read_b128 v[60:63], v1 offset:14336\n"
    "v_mfma_f32_16x16x32_bf16 v[138:141], v[76:79], a[108:111], v[138:141]\n"
    "s_waitcnt lgkmcnt(3)\n"
    "v_mfma_f32_16x16x32_bf16 v[126:129], a[104:107], v[80:83], v[126:129]\n"
    "ds_read_b128 v[64:67], v1 offset:28672\n"
    "v_mfma_f32_16x16x32_bf16 v[142:145], v[80:83], a[108:111], v[142:145]\n"
    "s_waitcnt lgkmcnt(3)\n"
    "v_mfma_f32_16x16x32_bf16 v[130:133], a[104:107], v[84:87], v[130:133]\n"
    "ds_read_b128 v[68:71], v1 offset:43008\n"
    "v_mfma_f32_16x16x32_bf16 v[146:149], v[84:87], a[108:111], v[146:149]\n"
    "v_mov_b32 v32, v202\n"
    "Llayer_19_%=:\n"
    "s_lshl_b32 s54, s21, 13\n"
    "s_add_u32 s54, s54, s35\n"
    "s_add_u32 s55, s54, 0x1000\n"
    "v_pk_fma_f32 v[118:119], v[110:111], v[24:25], v[118:119] op_sel_hi:[0,1,1] neg_lo:[1,0,0] neg_hi:[1,0,0]\n"
    "v_pk_fma_f32 v[120:121], v[110:111], v[26:27], v[120:121] op_sel_hi:[0,1,1] neg_lo:[1,0,0] neg_hi:[1,0,0]\n"
    "v_pk_fma_f32 v[122:123], v[112:113], v[24:25], v[122:123] op_sel_hi:[0,1,1] neg_lo:[1,0,0] neg_hi:[1,0,0]\n"
    "v_pk_fma_f32 v[124:125], v[112:113], v[26:27], v[124:125] op_sel_hi:[0,1,1] neg_lo:[1,0,0] neg_hi:[1,0,0]\n"
    "v_pk_mul_f32 v[118:119], v[110:111], v[118:119] op_sel:[1,0]\n"
    "v_pk_mul_f32 v[120:121], v[110:111], v[120:121] op_sel:[1,0]\n"
    "v_pk_mul_f32 v[122:123], v[112:113], v[122:123] op_sel:[1,0]\n"
    "v_pk_mul_f32 v[124:125], v[112:113], v[124:125] op_sel:[1,0]\n"
    "v_cvt_pk_bf16_f32 v198, v118, v119\n"
    "v_cvt_pk_bf16_f32 v199, v120, v121\n"
    "v_cvt_pk_bf16_f32 v200, v122, v123\n"
    "v_cvt_pk_bf16_f32 v201, v124, v125\n"
    "s_mov_b64 exec, s[44:45]\n"
    "v_mov_b32 v198, 0x3f800000\n"
    "v_mov_b32 v199, s42\n"
    "v_mov_b32 v200, 0x3f800000\n"
    "v_mov_b32 v201, s42\n"
    "s_mov_b64 exec, -1\n"
    "v_pk_fma_f32 v[134:135], v[166:167], v[32:33], v[134:135] op_sel_hi:[1,0,1] neg_lo:[1,0,0] neg_hi:[1,0,0]\n"
    "v_pk_fma_f32 v[136:137], v[168:169], v[32:33], v[136:137] op_sel_hi:[1,0,1] neg_lo:[1,0,0] neg_hi:[1,0,0]\n"
    "v_permlane16_swap_b32 v198, v200\n"
    "v_permlane16_swap_b32 v199, v201\n"
    "buffer_store_dwordx4 v[198:201], v42, s[8:11], s54 offen sc1\n"
    "v_pk_fma_f32 v[138:139], v[174:175], v[32:33], v[138:139] op_sel_hi:[1,0,1] neg_lo:[1,0,0] neg_hi:[1,0,0]\n"
    "v_pk_fma_f32 v[140:141], v[176:177], v[32:33], v[140:141] op_sel_hi:[1,0,1] neg_lo:[1,0,0] neg_hi:[1,0,0]\n"
    "v_pk_mul_f32 v[134:135], v[170:171], v[134:135]\n"
    "v_pk_mul_f32 v[136:137], v[172:173], v[136:137]\n"
    "v_pk_mul_f32 v[138:139], v[178:179], v[138:139]\n"
    "v_pk_mul_f32 v[140:141], v[180:181], v[140:141]\n"
    "v_cvt_pk_bf16_f32 v202, v134, v135\n"
    "v_cvt_pk_bf16_f32 v203, v136, v137\n"
    "v_cvt_pk_bf16_f32 v204, v138, v139\n"
    "v_cvt_pk_bf16_f32 v205, v140, v141\n"
    "s_mov_b64 exec, s[46:47]\n"
    "v_mov_b32 v202, s42\n"
    "v_mov_b32 v203, s42\n"
    "v_mov_b32 v204, s42\n"
    "v_mov_b32 v205, s42\n"
    "s_mov_b64 exec, -1\n"
    "buffer_store_dwordx4 v[202:205], v44, s[8:11], s54 offen sc1\n"
    "v_pk_fma_f32 v[126:127], v[114:115], v[24:25], v[126:127] op_sel_hi:[0,1,1] neg_lo:[1,0,0] neg_hi:[1,0,0]\n"
    "v_pk_fma_f32 v[128:129], v[114:115], v[26:27], v[128:129] op_sel_hi:[0,1,1] neg_lo:[1,0,0] neg_hi:[1,0,0]\n"
    "v_pk_fma_f32 v[130:131], v[116:117], v[24:25], v[130:131] op_sel_hi:[0,1,1] neg_lo:[1,0,0] neg_hi:[1,0,0]\n"
    "v_pk_fma_f32 v[132:133], v[116:117], v[26:27], v[132:133] op_sel_hi:[0,1,1] neg_lo:[1,0,0] neg_hi:[1,0,0]\n"
    "v_pk_mul_f32 v[126:127], v[114:115], v[126:127] op_sel:[1,0]\n"
    "v_pk_mul_f32 v[128:129], v[114:115], v[128:129] op_sel:[1,0]\n"
    "v_pk_mul_f32 v[130:131], v[116:117], v[130:131] op_sel:[1,0]\n"
    "v_pk_mul_f32 v[132:133], v[116:117], v[132:133] op_sel:[1,0]\n"
    "v_cvt_pk_bf16_f32 v206, v126, v127\n"
    "v_cvt_pk_bf16_f32 v207, v128, v129\n"
    "v_cvt_pk_bf16_f32 v208, v130, v131\n"
    "v_cvt_pk_bf16_f32 v209, v132, v133\n"
    "s_mov_b64 exec, s[44:45]\n"
    "v_mov_b32 v206, 0x3f800000\n"
    "v_mov_b32 v207, s42\n"
    "v_mov_b32 v208, 0x3f800000\n"
    "v_mov_b32 v209, s42\n"
    "s_mov_b64 exec, -1\n"
    "v_pk_fma_f32 v[142:143], v[182:183], v[32:33], v[142:143] op_sel_hi:[1,0,1] neg_lo:[1,0,0] neg_hi:[1,0,0]\n"
    "v_pk_fma_f32 v[144:145], v[184:185], v[32:33], v[144:145] op_sel_hi:[1,0,1] neg_lo:[1,0,0] neg_hi:[1,0,0]\n"
    "v_permlane16_swap_b32 v206, v208\n"
    "v_permlane16_swap_b32 v207, v209\n"
    "buffer_store_dwordx4 v[206:209], v42, s[8:11], s55 offen sc1\n"
    "v_pk_fma_f32 v[146:147], v[190:191], v[32:33], v[146:147] op_sel_hi:[1,0,1] neg_lo:[1,0,0] neg_hi:[1,0,0]\n"
    "v_pk_fma_f32 v[148:149], v[192:193], v[32:33], v[148:149] op_sel_hi:[1,0,1] neg_lo:[1,0,0] neg_hi:[1,0,0]\n"
    "v_pk_mul_f32 v[142:143], v[186:187], v[142:143]\n"
    "v_pk_mul_f32 v[144:145], v[188:189], v[144:145]\n"
    "v_pk_mul_f32 v[146:147], v[194:195], v[146:147]\n"
    "v_pk_mul_f32 v[148:149], v[196:197], v[148:149]\n"
    "v_cvt_pk_bf16_f32 v210, v142, v143\n"
    "v_cvt_pk_bf16_f32 v211, v144, v145\n"
    "v_cvt_pk_bf16_f32 v212, v146, v147\n"
    "v_cvt_pk_bf16_f32 v213, v148, v149\n"
    "s_mov_b64 exec, s[46:47]\n"
    "v_mov_b32 v210, s42\n"
    "v_mov_b32 v211, s42\n"
    "v_mov_b32 v212, s42\n"
    "v_mov_b32 v213, s42\n"
    "s_mov_b64 exec, -1\n"
    "buffer_store_dwordx4 v[210:213], v44, s[8:11], s55 offen sc1\n"
    "s_cmp_lg_u32 s23, 0\n"
    "s_cbranch_scc1 Lskip_20_%=\n"
    "s_add_u32 s71, s38, 0x68000\n"
    "buffer_load_dwordx2 v[46:47], v35, s[8:11], s71 offen sc1\n"
    "s_waitcnt vmcnt(0)\n"
    "v_cmp_ne_u64 vcc, s[26:27], v[46:47]\n"
    "s_nop 0\n"
    "s_cbranch_vccnz Lspin_21_%=\n"
    "Lgo_22_%=:\n"
    "Lskip_20_%=:\n"
    "; Q projection\n"
    "s_waitcnt vmcnt(17) lgkmcnt(3)\n"
    "v_mfma_f32_16x16x32_bf16 v[150:153], a[112:115], v[56:59], 0\n"
    "ds_read_b128 v[72:75], v1 offset:1024\n"
    "; signal 0: every wave has its write-through stores acknowledged; barrier; one wave stores the flag line\n"
    "s_waitcnt vmcnt(0)\n"
    "s_barrier\n"
    "s_cmp_lg_u32 s20, 0\n"
    "s_cbranch_scc1 Lskip_23_%=\n"
    "s_add_u32 s60, s39, 0x0\n"
    "s_add_u32 s60, s60, s38\n"
    "s_mov_b64 exec, 0xff\n"
    "buffer_store_dwordx4 v[36:39], v1, s[8:11], s60 offen sc1\n"
    "s_mov_b64 exec, -1\n"
    "Lskip_23_%=:\n"
    "buffer_load_dwordx4 a[168:171], v41, s[8:11], s29 offen offset:0 sc1\n"
    "s_waitcnt lgkmcnt(3)\n"
    "v_mfma_f32_16x16x32_bf16 v[154:157], a[112:115], v[60:63], 0\n"
    "ds_read_b128 v[76:79], v1 offset:15360\n"
    "buffer_load_dwordx4 a[172:175], v41, s[8:11], s29 offen offset:1024 sc1\n"
    "s_waitcnt lgkmcnt(3)\n"
    "v_mfma_f32_16x16x32_bf16 v[158:161], a[112:115], v[64:67], 0\n"
    "ds_read_b128 v[80:83], v1 offset:29696\n"
    "buffer_load_dwordx4 a[176:179], v41, s[8:11], s29 offen offset:2048 sc1\n"
    "s_waitcnt lgkmcnt(3)\n"
    "v_mfma_f32_16x16x32_bf16 v[162:165], a[112:115], v[68:71], 0\n"
    "ds_read_b128 v[84:87], v1 offset:44032\n"
    "buffer_load_dwordx4 a[180:183], v41, s[8:11], s29 offen offset:3072 sc1\n"
    "s_waitcnt lgkmcnt(3)\n"
    "v_mfma_f32_16x16x32_bf16 v[150:153], a[116:119], v[72:75], v[150:153]\n"
    "ds_read_b128 v[56:59], v1 offset:2048\n"
    "s_add_u32 s52, s29, 0x1000\n"
    "buffer_load_dwordx4 a[184:187], v41, s[8:11], s52 offen offset:0 sc1\n"
    "s_waitcnt lgkmcnt(3)\n"
    "v_mfma_f32_16x16x32_bf16 v[154:157], a[116:119], v[76:79], v[154:157]\n"
    "ds_read_b128 v[60:63], v1 offset:16384\n"
    "buffer_load_dwordx4 a[188:191], v41, s[8:11], s52 offen offset:1024 sc1\n"
    "s_waitcnt lgkmcnt(3)\n"
    "v_mfma_f32_16x16x32_bf16 v[158:161], a[116:119], v[80:83], v[158:161]\n"
    "ds_read_b128 v[64:67], v1 offset:30720\n"
    "buffer_load_dwordx4 a[192:195], v41, s[8:11], s52 offen offset:2048 sc1\n"
    "s_waitcnt lgkmcnt(3)\n"
    "v_mfma_f32_16x16x32_bf16 v[162:165], a[116:119], v[84:87], v[162:165]\n"
    "ds_read_b128 v[68:71], v1 offset:45056\n"
    "buffer_load_dwordx4 a[196:199], v41, s[8:11], s52 offen offset:3072 sc1\n"
    "s_waitcnt lgkmcnt(3)\n"
    "v_mfma_f32_16x16x32_bf16 v[150:153], a[120:123], v[56:59], v[150:153]\n"
    "ds_read_b128 v[72:75], v1 offset:3072\n"
    "s_add_u32 s52, s29, 0x2000\n"
    "buffer_load_dwordx4 a[200:203], v41, s[8:11], s52 offen offset:0 sc1\n"
    "s_waitcnt lgkmcnt(3)\n"
    "v_mfma_f32_16x16x32_bf16 v[154:157], a[120:123], v[60:63], v[154:157]\n"
    "ds_read_b128 v[76:79], v1 offset:17408\n"
    "s_waitcnt lgkmcnt(3)\n"
    "v_mfma_f32_16x16x32_bf16 v[158:161], a[120:123], v[64:67], v[158:161]\n"
    "ds_read_b128 v[80:83], v1 offset:31744\n"
    "s_waitcnt lgkmcnt(3)\n"
    "v_mfma_f32_16x16x32_bf16 v[162:165], a[120:123], v[68:71], v[162:165]\n"
    "ds_read_b128 v[84:87], v1 offset:46080\n"
    "s_waitcnt lgkmcnt(3)\n"
    "v_mfma_f32_16x16x32_bf16 v[150:153], a[124:127], v[72:75], v[150:153]\n"
    "ds_read_b128 v[56:59], v1 offset:4096\n"
    "s_waitcnt lgkmcnt(3)\n"
    "v_mfma_f32_16x16x32_bf16 v[154:157], a[124:127], v[76:79], v[154:157]\n"
    "ds_read_b128 v[60:63], v1 offset:18432\n"
    "s_waitcnt lgkmcnt(3)\n"
    "v_mfma_f32_16x16x32_bf16 v[158:161], a[124:127], v[80:83], v[158:161]\n"
    "ds_read_b128 v[64:67], v1 offset:32768\n"
    "s_waitcnt lgkmcnt(3)\n"
    "v_mfma_f32_16x16x32_bf16 v[162:165], a[124:127], v[84:87], v[162:165]\n"
    "ds_read_b128 v[68:71], v1 offset:47104\n"
    "s_waitcnt lgkmcnt(3)\n"
    "v_mfma_f32_16x16x32_bf16 v[150:153], a[128:131], v[56:59], v[150:153]\n"
    "ds_read_b128 v[72:75], v1 offset:5120\n"
    "s_waitcnt lgkmcnt(3)\n"
    "v_mfma_f32_16x16x32_bf16 v[154:157], a[128:131], v[60:63], v[154:157]\n"
    "ds_read_b128 v[76:79], v1 offset:19456\n"
    "s_waitcnt lgkmcnt(3)\n"
    "v_mfma_f32_16x16x32_bf16 v[158:161], a[128:131], v[64:67], v[158:161]\n"
    "ds_read_b128 v[80:83], v1 offset:33792\n"
    "s_waitcnt lgkmcnt(3)\n"
    "v_mfma_f32_16x16x32_bf16 v[162:165], a[128:131], v[68:71], v[162:165]\n"
    "ds_read_b128 v[84:87], v1 offset:48128\n"
    "s_waitcnt lgkmcnt(3)\n"
    "v_mfma_f32_16x16x32_bf16 v[150:153], a[132:135], v[72:75], v[150:153]\n"
    "ds_read_b128 v[56:59], v1 offset:6144\n"
    "s_waitcnt lgkmcnt(3)\n"
    "v_mfma_f32_16x16x32_bf16 v[154:157], a[132:135], v[76:79], v[154:157]\n"
    "ds_read_b128 v[60:63], v1 offset:20480\n"
    "s_waitcnt lgkmcnt(3)\n"
    "v_mfma_f32_16x16x32_bf16 v[158:161], a[132:135], v[80:83], v[158:161]\n"
    "ds_read_b128 v[64:67], v1 offset:34816\n"
    "s_waitcnt lgkmcnt(3)\n"
    "v_mfma_f32_16x16x32_bf16 v[162:165], a[132:135], v[84:87], v[162:165]\n"
    "ds_read_b128 v[68:71], v1 offset:49152\n"
    "s_waitcnt lgkmcnt(3)\n"
    "v_mfma_f32_16x16x32_bf16 v[150:153], a[136:139], v[56:59], v[150:153]\n"
    "ds_read_b128 v[72:75], v1 offset:7168\n"
    "s_waitcnt lgkmcnt(3)\n"
    "v_mfma_f32_16x16x32_bf16 v[154:157], a[136:139], v[60:63], v[154:157]\n"
    "ds_read_b128 v[76:79], v1 offset:21504\n"
    "s_waitcnt lgkmcnt(3)\n"
    "v_mfma_f32_16x16x32_bf16 v[158:161], a[136:139], v[64:67], v[158:161]\n"
    "ds_read_b128 v[80:83], v1 offset:35840\n"
    "s_waitcnt lgkmcnt(3)\n"
    "v_mfma_f32_16x16x32_bf16 v[162:165], a[136:139], v[68:71], v[162:165]\n"
    "ds_read_b128 v[84:87], v1 offset:50176\n"
    "s_waitcnt lgkmcnt(3)\n"
    "v_mfma_f32_16x16x32_bf16 v[150:153], a[140:143], v[72:75], v[150:153]\n"
    "ds_read_b128 v[56:59], v1 offset:8192\n"
    "s_waitcnt lgkmcnt(3)\n"
    "v_mfma_f32_16x16x32_bf16 v[154:157], a[140:143], v[76:79], v[154:157]\n"
    "ds_read_b128 v[60:63], v1 offset:22528\n"
    "s_waitcnt lgkmcnt(3)\n"
    "v_mfma_f32_16x16x32_bf16 v[158:161], a[140:143], v[80:83], v[158:161]\n"
    "ds_read_b128 v[64:67], v1 offset:36864\n"
    "buffer_load_dwordx4 a[204:207], v41, s[8:11], s52 offen offset:1024 sc1\n"
    "s_waitcnt lgkmcnt(3)\n"
    "v_mfma_f32_16x16x32_bf16 v[162:165], a[140:143], v[84:87], v[162:165]\n"
    "ds_read_b128 v[68:71], v1 offset:51200\n"
    "s_waitcnt lgkmcnt(3)\n"
    "v_mfma_f32_16x16x32_bf16 v[150:153], a[144:147], v[56:59], v[150:153]\n"
    "ds_read_b128 v[72:75], v1 offset:9216\n"
    "s_waitcnt lgkmcnt(3)\n"
    "v_mfma_f32_16x16x32_bf16 v[154:157], a[144:147], v[60:63], v[154:157]\n"
    "ds_read_b128 v[76:79], v1 offset:23552\n"
    "s_waitcnt lgkmcnt(3)\n"
    "v_mfma_f32_16x16x32_bf16 v[158:161], a[144:147], v[64:67], v[158:161]\n"
    "ds_read_b128 v[80:83], v1 offset:37888\n"
    "buffer_load_dwordx4 a[208:211], v41, s[8:11], s52 offen offset:2048 sc1\n"
    "s_waitcnt lgkmcnt(3)\n"
    "v_mfma_f32_16x16x32_bf16 v[162:165], a[144:147], v[68:71], v[162:165]\n"
    "ds_read_b128 v[84:87], v1 offset:52224\n"
    "s_waitcnt lgkmcnt(3)\n"
    "v_mfma_f32_16x16x32_bf16 v[150:153], a[148:151], v[72:75], v[150:153]\n"
    "ds_read_b128 v[56:59], v1 offset:10240\n"
    "s_waitcnt lgkmcnt(3)\n"
    "v_mfma_f32_16x16x32_bf16 v[154:157], a[148:151], v[76:79], v[154:157]\n"
    "ds_read_b128 v[60:63], v1 offset:24576\n"
    "s_add_u32 s71, s38, 0x0\n"
    "buffer_load_dwordx2 v[46:47], v40, s[8:11], s71 offen sc1\n"
    "buffer_load_dwordx4 a[212:215], v41, s[8:11], s52 offen offset:3072 sc1\n"
    "s_waitcnt lgkmcnt(3)\n"
    "v_mfma_f32_16x16x32_bf16 v[158:161], a[148:151], v[80:83], v[158:161]\n"
    "ds_read_b128 v[64:67], v1 offset:38912\n"
    "s_waitcnt lgkmcnt(3)\n"
    "v_mfma_f32_16x16x32_bf16 v[162:165], a[148:151], v[84:87], v[162:165]\n"
    "ds_read_b128 v[68:71], v1 offset:53248\n"
    "s_add_u32 s52, s29, 0x3000\n"
    "buffer_load_dwordx4 a[216:219], v41, s[8:11], s52 offen offset:0 sc1\n"
    "s_waitcnt lgkmcnt(3)\n"
    "v_mfma_f32_16x16x32_bf16 v[150:153], a[152:155], v[56:59], v[150:153]\n"
    "ds_read_b128 v[72:75], v1 offset:11264\n"
    "s_waitcnt lgkmcnt(3)\n"
    "v_mfma_f32_16x16x32_bf16 v[154:157], a[152:155], v[60:63], v[154:157]\n"
    "ds_read_b128 v[76:79], v1 offset:25600\n"
    "s_waitcnt lgkmcnt(3)\n"
    "v_mfma_f32_16x16x32_bf16 v[158:161], a[152:155], v[64:67], v[158:161]\n"
    "ds_read_b128 v[80:83], v1 offset:39936\n"
    "s_waitcnt lgkmcnt(3)\n"
    "v_mfma_f32_16x16x32_bf16 v[162:165], a[152:155], v[68:71], v[162:165]\n"
    "ds_read_b128 v[84:87], v1 offset:54272\n"
    "buffer_load_dwordx4 a[220:223], v41, s[8:11], s52 offen offset:1024 sc1\n"
    "s_waitcnt lgkmcnt(3)\n"
    "v_mfma_f32_16x16x32_bf16 v[150:153], a[156:159], v[72:75], v[150:153]\n"
    "ds_read_b128 v[56:59], v1 offset:12288\n"
    "s_waitcnt lgkmcnt(3)\n"
    "v_mfma_f32_16x16x32_bf16 v[154:157], a[156:159], v[76:79], v[154:157]\n"
    "ds_read_b128 v[60:63], v1 offset:26624\n"
    "s_waitcnt lgkmcnt(3)\n"
    "v_mfma_f32_16x16x32_bf16 v[158:161], a[156:159], v[80:83], v[158:161]\n"
    "ds_read_b128 v[64:67], v1 offset:40960\n"
    "buffer_load_dwordx4 a[224:227], v41, s[8:11], s52 offen offset:2048 sc1\n"
    "s_waitcnt lgkmcnt(3)\n"
    "v_mfma_f32_16x16x32_bf16 v[162:165], a[156:159], v[84:87], v[162:165]\n"
    "ds_read_b128 v[68:71], v1 offset:55296\n"
    "s_waitcnt lgkmcnt(3)\n"
    "v_mfma_f32_16x16x32_bf16 v[150:153], a[160:163], v[56:59], v[150:153]\n"
    "ds_read_b128 v[72:75], v1 offset:13312\n"
    "s_waitcnt lgkmcnt(3)\n"
    "v_mfma_f32_16x16x32_bf16 v[154:157], a[160:163], v[60:63], v[154:157]\n"
    "ds_read_b128 v[76:79], v1 offset:27648\n"
    "s_waitcnt lgkmcnt(3)\n"
    "v_mfma_f32_16x16x32_bf16 v[158:161], a[160:163], v[64:67], v[158:161]\n"
    "ds_read_b128 v[80:83], v1 offset:41984\n"
    "buffer_load_dwordx4 a[228:231], v41, s[8:11], s52 offen offset:3072 sc1\n"
    "s_waitcnt lgkmcnt(3)\n"
    "v_mfma_f32_16x16x32_bf16 v[162:165], a[160:163], v[68:71], v[162:165]\n"
    "ds_read_b128 v[84:87], v1 offset:56320\n"
    "s_waitcnt lgkmcnt(3)\n"
    "v_mfma_f32_16x16x32_bf16 v[150:153], a[164:167], v[72:75], v[150:153]\n"
    "s_waitcnt lgkmcnt(2)\n"
    "v_mfma_f32_16x16x32_bf16 v[154:157], a[164:167], v[76:79], v[154:157]\n"
    "buffer_load_dwordx4 a[232:235], v1, s[8:11], s30 offen offset:0 sc1\n"
    "s_waitcnt lgkmcnt(1)\n"
    "v_mfma_f32_16x16x32_bf16 v[158:161], a[164:167], v[80:83], v[158:161]\n"
    "s_waitcnt lgkmcnt(0)\n"
    "v_mfma_f32_16x16x32_bf16 v[162:165], a[164:167], v[84:87], v[162:165]\n"
    "s_cmp_lg_u32 s23, 0\n"
    "s_cbranch_scc1 Lskip_24_%=\n"
    "v_mov_b32 v206, s42\n"
    "v_mov_b32 v207, s42\n"
    "v_mov_b32 v208, s42\n"
    "v_mov_b32 v209, s42\n"
    "s_nop 1\n"
    "v_mfma_f32_16x16x32_bf16 v[28:31], a[112:115], v[206:209], 0\n"
    "v_mfma_f32_16x16x32_bf16 v[28:31], a[116:119], v[206:209], v[28:31]\n"
    "v_mfma_f32_16x16x32_bf16 v[28:31], a[120:123], v[206:209], v[28:31]\n"
    "v_mfma_f32_16x16x32_bf16 v[28:31], a[124:127], v[206:209], v[28:31]\n"
    "v_mfma_f32_16x16x32_bf16 v[28:31], a[128:131], v[206:209], v[28:31]\n"
    "v_mfma_f32_16x16x32_bf16 v[28:31], a[132:135], v[206:209], v[28:31]\n"
    "v_mfma_f32_16x16x32_bf16 v[28:31], a[136:139], v[206:209], v[28:31]\n"
    "v_mfma_f32_16x16x32_bf16 v[28:31], a[140:143], v[206:209], v[28:31]\n"
    "v_mfma_f32_16x16x32_bf16 v[28:31], a[144:147], v[206:209], v[28:31]\n"
    "v_mfma_f32_16x16x32_bf16 v[28:31], a[148:151], v[206:209], v[28:31]\n"
    "v_mfma_f32_16x16x32_bf16 v[28:31], a[152:155], v[206:209], v[28:31]\n"
    "v_mfma_f32_16x16x32_bf16 v[28:31], a[156:159], v[206:209], v[28:31]\n"
    "v_mfma_f32_16x16x32_bf16 v[28:31], a[160:163], v[206:209], v[28:31]\n"
    "v_mfma_f32_16x16x32_bf16 v[28:31], a[164:167], v[206:209], v[28:31]\n"
    "Lskip_24_%=:\n"
    "v_mul_f32 v111, 0x3e8b97f6, v111\n"
    "v_mul_f32 v113, 0x3e8b97f6, v113\n"
    "v_mul_f32 v115, 0x3e8b97f6, v115\n"
    "v_mul_f32 v117, 0x3e8b97f6, v117\n"
    "s_nop 3\n"
    "v_pk_fma_f32 v[150:151], v[110:111], v[28:29], v[150:151] op_sel_hi:[0,1,1] neg_lo:[1,0,0] neg_hi:[1,0,0]\n"
    "v_pk_fma_f32 v[152:153], v[110:111], v[30:31], v[152:153] op_sel_hi:[0,1,1] neg_lo:[1,0,0] neg_hi:[1,0,0]\n"
    "v_pk_fma_f32 v[154:155], v[112:113], v[28:29], v[154:155] op_sel_hi:[0,1,1] neg_lo:[1,0,0] neg_hi:[1,0,0]\n"
    "v_pk_fma_f32 v[156:157], v[112:113], v[30:31], v[156:157] op_sel_hi:[0,1,1] neg_lo:[1,0,0] neg_hi:[1,0,0]\n"
    "v_pk_mul_f32 v[150:151], v[110:111], v[150:151] op_sel:[1,0]\n"
    "v_pk_mul_f32 v[152:153], v[110:111], v[152:153] op_sel:[1,0]\n"
    "v_pk_mul_f32 v[154:155], v[112:113], v[154:155] op_sel:[1,0]\n"
    "v_pk_mul_f32 v[156:157], v[112:113], v[156:157] op_sel:[1,0]\n"
    "v_cvt_pk_bf16_f32 v198, v150, v151\n"
    "v_cvt_pk_bf16_f32 v199, v152, v153\n"
    "v_cvt_pk_bf16_f32 v200, v154, v155\n"
    "v_cvt_pk_bf16_f32 v201, v156, v157\n"
    "s_mov_b64 exec, s[44:45]\n"
    "v_mov_b32 v198, 0x3f80\n"
    "v_mov_b32 v200, 0x3f80\n"
    "s_mov_b64 exec, -1\n"
    "s_nop 0\n"
    "v_permlane16_swap_b32 v198, v200\n"
    "v_permlane16_swap_b32 v199, v201\n"
    "ds_write_b128 v52, v[198:201] offset:0\n"
    "v_pk_fma_f32 v[158:159], v[114:115], v[28:29], v[158:159] op_sel_hi:[0,1,1] neg_lo:[1,0,0] neg_hi:[1,0,0]\n"
    "v_pk_fma_f32 v[160:161], v[114:115], v[30:31], v[160:161] op_sel_hi:[0,1,1] neg_lo:[1,0,0] neg_hi:[1,0,0]\n"
    "v_pk_fma_f32 v[162:163], v[116:117], v[28:29], v[162:163] op_sel_hi:[0,1,1] neg_lo:[1,0,0] neg_hi:[1,0,0]\n"
    "v_pk_fma_f32 v[164:165], v[116:117], v[30:31], v[164:165] op_sel_hi:[0,1,1] neg_lo:[1,0,0] neg_hi:[1,0,0]\n"
    "v_pk_mul_f32 v[158:159], v[114:115], v[158:159] op_sel:[1,0]\n"
    "v_pk_mul_f32 v[160:161], v[114:115], v[160:161] op_sel:[1,0]\n"
    "v_pk_mul_f32 v[162:163], v[116:117], v[162:163] op_sel:[1,0]\n"
    "v_pk_mul_f32 v[164:165], v[116:117], v[164:165] op_sel:[1,0]\n"
    "v_cvt_pk_bf16_f32 v202, v158, v159\n"
    "v_cvt_pk_bf16_f32 v203, v160, v161\n"
    "v_cvt_pk_bf16_f32 v204, v162, v163\n"
    "v_cvt_pk_bf16_f32 v205, v164, v165\n"
    "s_mov_b64 exec, s[44:45]\n"
    "v_mov_b32 v202, 0x3f80\n"
    "v_mov_b32 v204, 0x3f80\n"
    "s_mov_b64 exec, -1\n"
    "s_nop 0\n"
    "v_permlane16_swap_b32 v202, v204\n"
    "v_permlane16_swap_b32 v203, v205\n"
    "ds_write_b128 v52, v[202:205] offset:2048\n"
    "s_waitcnt lgkmcnt(0)\n"
    "s_barrier\n"
    "ds_read_b128 v[136:139], v53 offset:0\n"
    "ds_read_b128 v[140:143], v53 offset:1024\n"
    "ds_read_b128 v[144:147], v54 offset:0\n"
    "ds_read_b128 v[148:151], v54 offset:1024\n"
    "ds_read_b128 v[152:155], v34 offset:0\n"
    "ds_read_b128 v[156:159], v34 offset:1024\n"
    "ds_read_b128 v[160:163], v34 offset:2048\n"
    "ds_read_b128 v[164:167], v34 offset:3072\n"
    "s_add_u32 s72, s77, s36\n"
    "s_waitcnt vmcnt(6)\n"
    "v_cmp_ne_u64 vcc, s[26:27], v[46:47]\n"
    "s_nop 0\n"
    "s_cbranch_vccnz Lspin_25_%=\n"
    "Lgo_26_%=:\n"
    "; attention, first pass: row maxima\n"
    "buffer_load_dwordx4 v[56:59], v1, s[8:11], s66 offen offset:0 sc1\n"
    "buffer_load_dwordx4 v[60:63], v1, s[8:11], s66 offen offset:1024 sc1\n"
    "buffer_load_dwordx4 v[64:67], v1, s[8:11], s67 offen offset:0 sc1\n"
    "buffer_load_dwordx4 v[68:71], v1, s[8:11], s67 offen offset:1024 sc1\n"
    "buffer_load_dwordx4 v[72:75], v1, s[8:11], s68 offen offset:0 sc1\n"
    "buffer_load_dwordx4 v[76:79], v1, s[8:11], s68 offen offset:1024 sc1\n"
    "buffer_load_dwordx4 v[80:83], v1, s[8:11], s69 offen offset:0 sc1\n"
    "buffer_load_dwordx4 v[84:87], v1, s[8:11], s69 offen offset:1024 sc1\n"
    "buffer_load_dwordx4 v[88:91], v1, s[8:11], s70 offen offset:0 sc1\n"
    "s_waitcnt vmcnt(8) lgkmcnt(0)\n"
    "v_mfma_f32_32x32x16_bf16 v[168:183], v[56:59], v[136:139], v[152:167]\n"
    "buffer_load_dwordx4 v[92:95], v1, s[8:11], s70 offen offset:1024 sc1\n"
    "s_waitcnt vmcnt(8)\n"
    "v_mfma_f32_32x32x16_bf16 v[168:183], v[60:63], v[140:143], v[168:183]\n"
    "buffer_load_dwordx4 v[104:107], v1, s[8:11], s67 offen offset:2048 sc1\n"
    "s_waitcnt vmcnt(8)\n"
    "v_mfma_f32_32x32x16_bf16 v[184:199], v[64:67], v[136:139], 0\n"
    "buffer_load_dwordx4 v[108:111], v1, s[8:11], s67 offen offset:3072 sc1\n"
    "s_waitcnt vmcnt(8)\n"
    "v_mfma_f32_32x32x16_bf16 v[184:199], v[68:71], v[140:143], v[184:199]\n"
    "buffer_load_dwordx4 v[112:115], v1, s[8:11], s68 offen offset:2048 sc1\n"
    "v_mfma_f32_32x32x16_bf16 v[200:215], v[64:67], v[144:147], v[152:167]\n"
    "s_nop 3\n"
    "v_max_f32 v248, v168, v169\n"
    "v_max3_f32 v248, v248, v170, v171\n"
    "v_max3_f32 v248, v248, v172, v173\n"
    "v_max3_f32 v248, v248, v174, v175\n"
    "v_mfma_f32_32x32x16_bf16 v[200:215], v[68:71], v[148:151], v[200:215]\n"
    "v_max3_f32 v248, v248, v176, v177\n"
    "v_max3_f32 v248, v248, v178, v179\n"
    "v_max3_f32 v248, v248, v180, v181\n"
    "v_max3_f32 v248, v248, v182, v183\n"
    "s_waitcnt vmcnt(8)\n"
    "v_mfma_f32_32x32x16_bf16 v[168:183], v[72:75], v[136:139], 0\n"
    "buffer_load_dwordx4 v[116:119], v1, s[8:11], s68 offen offset:3072 sc1\n"
    "v_max3_f32 v248, v248, v184, v185\n"
    "v_max3_f32 v248, v248, v186, v187\n"
    "v_max3_f32 v248, v248, v188, v189\n"
    "v_max3_f32 v248, v248, v190, v191\n"
    "s_waitcnt vmcnt(8)\n"
    "v_mfma_f32_32x32x16_bf16 v[168:183], v[76:79], v[140:143], v[168:183]\n"
    "buffer_load_dwordx4 v[120:123], v1, s[8:11], s69 offen offset:2048 sc1\n"
    "v_max3_f32 v248, v248, v192, v193\n"
    "v_max3_f32 v248, v248, v194, v195\n"
    "v_max3_f32 v248, v248, v196, v197\n"
    "v_max3_f32 v248, v248, v198, v199\n"
    "v_mfma_f32_32x32x16_bf16 v[184:199], v[72:75], v[144:147], 0\n"
    "v_max_f32 v249, v200, v201\n"
    "v_max3_f32 v249, v249, v202, v203\n"
    "v_max3_f32 v249, v249, v204, v205\n"
    "v_max3_f32 v249, v249, v206, v207\n"
    "v_mfma_f32_32x32x16_bf16 v[184:199], v[76:79], v[148:151], v[184:199]\n"
    "buffer_load_dwordx4 v[124:127], v1, s[8:11], s69 offen offset:3072 sc1\n"
    "v_max3_f32 v249, v249, v208, v209\n"
    "v_max3_f32 v249, v249, v210, v211\n"
    "v_max3_f32 v249, v249, v212, v213\n"
    "v_max3_f32 v249, v249, v214, v215\n"
    "s_waitcnt vmcnt(9)\n"
    "v_mfma_f32_32x32x16_bf16 v[200:215], v[80:83], v[136:139], 0\n"
    "v_max3_f32 v248, v248, v168, v169\n"
    "v_max3_f32 v248, v248, v170, v171\n"
    "v_max3_f32 v248, v248, v172, v173\n"
    "v_max3_f32 v248, v248, v174, v175\n"
    "s_waitcnt vmcnt(8)\n"
    "v_mfma_f32_32x32x16_bf16 v[200:215], v[84:87], v[140:143], v[200:215]\n"
    "buffer_load_dwordx4 v[128:131], v1, s[8:11], s70 offen offset:2048 sc1\n"
    "v_max3_f32 v248, v248, v176, v177\n"
    "v_max3_f32 v248, v248, v178, v179\n"
    "v_max3_f32 v248, v248, v180, v181\n"
    "v_max3_f32 v248, v248, v182, v183\n"
    "v_mfma_f32_32x32x16_bf16 v[168:183], v[80:83], v[144:147], 0\n"
    "v_max3_f32 v249, v249, v184, v185\n"
    "v_max3_f32 v249, v249, v186, v187\n"
    "v_max3_f32 v249, v249, v188, v189\n"
    "v_max3_f32 v249, v249, v190, v191\n"
    "v_mfma_f32_32x32x16_bf16 v[168:183], v[84:87], v[148:151], v[168:183]\n"
    "buffer_load_dwordx4 v[132:135], v1, s[8:11], s70 offen offset:3072 sc1\n"
    "v_max3_f32 v249, v249, v192, v193\n"
    "v_max3_f32 v249, v249, v194, v195\n"
    "v_max3_f32 v249, v249, v196, v197\n"
    "v_max3_f32 v249, v249, v198, v199\n"
    "s_waitcnt vmcnt(9)\n"
    "v_mfma_f32_32x32x16_bf16 v[184:199], v[88:91], v[136:139], 0\n"
    "v_max3_f32 v248, v248, v200, v201\n"
    "v_max3_f32 v248, v248, v202, v203\n"
    "v_max3_f32 v248, v248, v204, v205\n"
    "v_max3_f32 v248, v248, v206, v207\n"
    "s_waitcnt vmcnt(8)\n"
    "v_mfma_f32_32x32x16_bf16 v[184:199], v[92:95], v[140:143], v[184:199]\n"
    "buffer_load_dwordx4 v[96:99], v1, s[8:11], s66 offen offset:2048 sc1\n"
    "v_max3_f32 v248, v248, v208, v209\n"
    "v_max3_f32 v248, v248, v210, v211\n"
    "v_max3_f32 v248, v248, v212, v213\n"
    "v_max3_f32 v248, v248, v214, v215\n"
    "v_mfma_f32_32x32x16_bf16 v[200:215], v[88:91], v[144:147], 0\n"
    "v_max3_f32 v249, v249, v168, v169\n"
    "v_max3_f32 v249, v249, v170, v171\n"
    "v_max3_f32 v249, v249, v172, v173\n"
    "v_max3_f32 v249, v249, v174, v175\n"
    "v_mfma_f32_32x32x16_bf16 v[200:215], v[92:95], v[148:151], v[200:215]\n"
    "buffer_load_dwordx4 v[100:103], v1, s[8:11], s66 offen offset:3072 sc1\n"
    "v_max3_f32 v249, v249, v176, v177\n"
    "v_max3_f32 v249, v249, v178, v179\n"
    "v_max3_f32 v249, v249, v180, v181\n"
    "v_max3_f32 v249, v249, v182, v183\n"
    "v_max3_f32 v248, v248, v184, v185\n"
    "v_max3_f32 v248, v248, v186, v187\n"
    "v_max3_f32 v248, v248, v188, v189\n"
    "v_max3_f32 v248, v248, v190, v191\n"
    "buffer_load_dwordx4 a[236:239], v1, s[8:11], s30 offen offset:1024 sc1\n"
    "v_max3_f32 v248, v248, v192, v193\n"
    "v_max3_f32 v248, v248, v194, v195\n"
    "v_max3_f32 v248, v248, v196, v197\n"
    "v_max3_f32 v248, v248, v198, v199\n"
    "v_max3_f32 v249, v249, v200, v201\n"
    "v_max3_f32 v249, v249, v202, v203\n"
    "v_max3_f32 v249, v249, v204, v205\n"
    "v_max3_f32 v249, v249, v206, v207\n"
    "v_max3_f32 v249, v249, v208, v209\n"
    "v_max3_f32 v249, v249, v210, v211\n"
    "v_max3_f32 v249, v249, v212, v213\n"
    "v_max3_f32 v249, v249, v214, v215\n"
    "; fold -max into the padding dimensions 29..31 of Q (three bf16 pieces = the exact f32 value)\n"
    "v_mov_b32 v209, v249\n"
    "v_mov_b32 v208, v248\n"
    "s_nop 0\n"
    "v_permlane32_swap_b32 v249, v209\n"
    "v_permlane32_swap_b32 v248, v208\n"
    "v_max_f32 v249, v249, v209\n"
    "v_max_f32 v248, v248, v208\n"
    "s_mov_b64 exec, s[48:49]\n"
    "v_sub_f32 v212, 0, v249\n"
    "v_and_b32 v213, s51, v212\n"
    "v_sub_f32 v212, v212, v213\n"
    "v_or_b32 v150, 0x3f80, v213\n"
    "v_and_b32 v214, s51, v212\n"
    "v_sub_f32 v212, v212, v214\n"
    "v_perm_b32 v151, v212, v214, s50\n"
    "v_sub_f32 v208, 0, v248\n"
    "v_and_b32 v209, s51, v208\n"
    "v_sub_f32 v208, v208, v209\n"
    "v_or_b32 v142, 0x3f80, v209\n"
    "v_and_b32 v210, s51, v208\n"
    "v_sub_f32 v208, v208, v210\n"
    "v_perm_b32 v143, v208, v210, s50\n"
    "s_mov_b64 exec, -1\n"
    "; second pass\n"
    "v_mfma_f32_32x32x16_bf16 v[168:183], v[64:67], v[144:147], v[152:167]\n"
    "v_mfma_f32_32x32x16_bf16 v[168:183], v[68:71], v[148:151], v[168:183]\n"
    "v_mfma_f32_32x32x16_bf16 v[184:199], v[72:75], v[144:147], 0\n"
    "s_nop 10\n"
    "v_exp_f32 v168, v168\n"
    "v_exp_f32 v169, v169\n"
    "v_exp_f32 v170, v170\n"
    "v_exp_f32 v171, v171\n"
    "v_exp_f32 v172, v172\n"
    "v_exp_f32 v173, v173\n"
    "v_exp_f32 v174, v174\n"
    "v_exp_f32 v175, v175\n"
    "buffer_load_dwordx4 a[240:243], v1, s[8:11], s30 offen offset:2048 sc1\n"
    "v_cvt_pk_bf16_f32 v200, v168, v169\n"
    "v_cvt_pk_bf16_f32 v201, v170, v171\n"
    "v_mfma_f32_32x32x16_bf16 v[184:199], v[76:79], v[148:151], v[184:199]\n"
    "v_cvt_pk_bf16_f32 v202, v172, v173\n"
    "v_cvt_pk_bf16_f32 v203, v174, v175\n"
    "v_exp_f32 v176, v176\n"
    "v_exp_f32 v177, v177\n"
    "v_exp_f32 v178, v178\n"
    "v_exp_f32 v179, v179\n"
    "s_waitcnt vmcnt(11)\n"
    "v_mfma_f32_32x32x16_bf16 v[232:247], v[104:107], v[200:203], 0\n"
    "v_exp_f32 v180, v180\n"
    "v_exp_f32 v181, v181\n"
    "v_exp_f32 v182, v182\n"
    "v_exp_f32 v183, v183\n"
    "buffer_load_dwordx4 a[244:247], v1, s[8:11], s30 offen offset:3072 sc1\n"
    "v_cvt_pk_bf16_f32 v204, v176, v177\n"
    "v_cvt_pk_bf16_f32 v205, v178, v179\n"
    "v_cvt_pk_bf16_f32 v206, v180, v181\n"
    "v_cvt_pk_bf16_f32 v207, v182, v183\n"
    "v_mfma_f32_32x32x16_bf16 v[168:183], v[80:83], v[144:147], 0\n"
    "v_exp_f32 v184, v184\n"
    "v_exp_f32 v185, v185\n"
    "v_exp_f32 v186, v186\n"
    "v_exp_f32 v187, v187\n"
    "s_waitcnt vmcnt(11)\n"
    "v_mfma_f32_32x32x16_bf16 v[232:247], v[108:111], v[204:207], v[232:247]\n"
    "v_exp_f32 v188, v188\n"
    "v_exp_f32 v189, v189\n"
    "v_exp_f32 v190, v190\n"
    "v_exp_f32 v191, v191\n"
    "s_add_u32 s52, s30, 0x1000\n"
    "buffer_load_dwordx4 a[248:251], v1, s[8:11], s52 offen offset:0 sc1\n"
    "v_cvt_pk_bf16_f32 v200, v184, v185\n"
    "v_cvt_pk_bf16_f32 v201, v186, v187\n"
    "v_mfma_f32_32x32x16_bf16 v[168:183], v[84:87], v[148:151], v[168:183]\n"
    "v_cvt_pk_bf16_f32 v202, v188, v189\n"
    "v_cvt_pk_bf16_f32 v203, v190, v191\n"
    "v_exp_f32 v192, v192\n"
    "v_exp_f32 v193, v193\n"
    "v_exp_f32 v194, v194\n"
    "v_exp_f32 v195, v195\n"
    "s_waitcnt vmcnt(11)\n"
    "v_mfma_f32_32x32x16_bf16 v[232:247], v[112:115], v[200:203], v[232:247]\n"
    "v_exp_f32 v196, v196\n"
    "v_exp_f32 v197, v197\n"
    "v_exp_f32 v198, v198\n"
    "v_exp_f32 v199, v199\n"
    "buffer_load_dwordx4 a[252:255], v1, s[8:11], s52 offen offset:1024 sc1\n"
    "v_cvt_pk_bf16_f32 v204, v192, v193\n"
    "v_cvt_pk_bf16_f32 v205, v194, v195\n"
    "v_cvt_pk_bf16_f32 v206, v196, v197\n"
    "v_cvt_pk_bf16_f32 v207, v198, v199\n"
    "v_mfma_f32_32x32x16_bf16 v[184:199], v[88:91], v[144:147], 0\n"
    "v_exp_f32 v168, v168\n"
    "v_exp_f32 v169, v169\n"
    "v_exp_f32 v170, v170\n"
    "v_exp_f32 v171, v171\n"
    "s_waitcnt vmcnt(11)\n"
    "v_mfma_f32_32x32x16_bf16 v[232:247], v[116:119], v[204:207], v[232:247]\n"
    "v_exp_f32 v172, v172\n"
    "v_exp_f32 v173, v173\n"
    "v_exp_f32 v174, v174\n"
    "v_exp_f32 v175, v175\n"
    "buffer_load_dwordx4 a[32:35], v1, s[8:11], s52 offen offset:2048 sc1\n"
    "v_cvt_pk_bf16_f32 v200, v168, v169\n"
    "v_cvt_pk_bf16_f32 v201, v170, v171\n"
    "v_mfma_f32_32x32x16_bf16 v[184:199], v[92:95], v[148:151], v[184:199]\n"
    "v_cvt_pk_bf16_f32 v202, v172, v173\n"
    "v_cvt_pk_bf16_f32 v203, v174, v175\n"
    "v_exp_f32 v176, v176\n"
    "v_exp_f32 v177, v177\n"
    "v_exp_f32 v178, v178\n"
    "v_exp_f32 v179, v179\n"
    "s_waitcnt vmcnt(11)\n"
    "v_mfma_f32_32x32x16_bf16 v[232:247], v[120:123], v[200:203], v[232:247]\n"
    "v_exp_f32 v180, v180\n"
    "v_exp_f32 v181, v181\n"
    "v_exp_f32 v182, v182\n"
    "v_exp_f32 v183, v183\n"
    "buffer_load_dwordx4 a[36:39], v1, s[8:11], s52 offen offset:3072 sc1\n"
    "v_cvt_pk_bf16_f32 v204, v176, v177\n"
    "v_cvt_pk_bf16_f32 v205, v178, v179\n"
    "v_cvt_pk_bf16_f32 v206, v180, v181\n"
    "v_cvt_pk_bf16_f32 v207, v182, v183\n"
    "v_mfma_f32_32x32x16_bf16 v[168:183], v[56:59], v[136:139], v[152:167]\n"
    "v_exp_f32 v184, v184\n"
    "v_exp_f32 v185, v185\n"
    "v_exp_f32 v186, v186\n"
    "v_exp_f32 v187, v187\n"
    "s_waitcnt vmcnt(11)\n"
    "v_mfma_f32_32x32x16_bf16 v[232:247], v[124:127], v[204:207], v[232:247]\n"
    "v_exp_f32 v188, v188\n"
    "v_exp_f32 v189, v189\n"
    "v_exp_f32 v190, v190\n"
    "v_exp_f32 v191, v191\n"
    "s_add_u32 s52, s30, 0x2000\n"
    "buffer_load_dwordx4 a[40:43], v1, s[8:11], s52 offen offset:0 sc1\n"
    "v_cvt_pk_bf16_f32 v200, v184, v185\n"
    "v_cvt_pk_bf16_f32 v201, v186, v187\n"
    "v_mfma_f32_32x32x16_bf16 v[168:183], v[60:63], v[140:143], v[168:183]\n"
    "v_cvt_pk_bf16_f32 v202, v188, v189\n"
    "v_cvt_pk_bf16_f32 v203, v190, v191\n"
    "v_exp_f32 v192, v192\n"
    "v_exp_f32 v193, v193\n"
    "v_exp_f32 v194, v194\n"
    "v_exp_f32 v195, v195\n"
    "s_waitcnt vmcnt(11)\n"
    "v_mfma_f32_32x32x16_bf16 v[232:247], v[128:131], v[200:203], v[232:247]\n"
    "v_exp_f32 v196, v196\n"
    "v_exp_f32 v197, v197\n"
    "v_exp_f32 v198, v198\n"
    "v_exp_f32 v199, v199\n"
    "buffer_load_dwordx4 a[44:47], v1, s[8:11], s52 offen offset:1024 sc1\n"
    "v_cvt_pk_bf16_f32 v204, v192, v193\n"
    "v_cvt_pk_bf16_f32 v205, v194, v195\n"
    "v_cvt_pk_bf16_f32 v206, v196, v197\n"
    "v_cvt_pk_bf16_f32 v207, v198, v199\n"
    "v_mfma_f32_32x32x16_bf16 v[184:199], v[64:67], v[136:139], 0\n"
    "v_exp_f32 v168, v168\n"
    "v_exp_f32 v169, v169\n"
    "v_exp_f32 v170, v170\n"
    "v_exp_f32 v171, v171\n"
    "s_waitcnt vmcnt(11)\n"
    "v_mfma_f32_32x32x16_bf16 v[232:247], v[132:135], v[204:207], v[232:247]\n"
    "v_exp_f32 v172, v172\n"
    "v_exp_f32 v173, v173\n"
    "v_exp_f32 v174, v174\n"
    "v_exp_f32 v175, v175\n"
    "buffer_load_dwordx4 a[48:51], v1, s[8:11], s52 offen offset:2048 sc1\n"
    "v_cvt_pk_bf16_f32 v200, v168, v169\n"
    "v_cvt_pk_bf16_f32 v201, v170, v171\n"
    "v_mfma_f32_32x32x16_bf16 v[184:199], v[68:71], v[140:143], v[184:199]\n"
    "v_cvt_pk_bf16_f32 v202, v172, v173\n"
    "v_cvt_pk_bf16_f32 v203, v174, v175\n"
    "v_exp_f32 v176, v176\n"
    "v_exp_f32 v177, v177\n"
    "v_exp_f32 v178, v178\n"
    "v_exp_f32 v179, v179\n"
    "s_waitcnt vmcnt(11)\n"
    "v_mfma_f32_32x32x16_bf16 v[216:231], v[96:99], v[200:203], 0\n"
    "v_exp_f32 v180, v180\n"
    "v_exp_f32 v181, v181\n"
    "v_exp_f32 v182, v182\n"
    "v_exp_f32 v183, v183\n"
    "ds_write_b32 v254, v249 offset:4096\n"
    "ds_write_b128 v254, v[244:247] offset:3072\n"
    "buffer_load_dwordx4 a[52:55], v1, s[8:11], s52 offen offset:3072 sc1\n"
    "v_cvt_pk_bf16_f32 v204, v176, v177\n"
    "v_cvt_pk_bf16_f32 v205, v178, v179\n"
    "v_cvt_pk_bf16_f32 v206, v180, v181\n"
    "v_cvt_pk_bf16_f32 v207, v182, v183\n"
    "v_mfma_f32_32x32x16_bf16 v[168:183], v[72:75], v[136:139], 0\n"
    "v_exp_f32 v184, v184\n"
    "v_exp_f32 v185, v185\n"
    "v_exp_f32 v186, v186\n"
    "v_exp_f32 v187, v187\n"
    "s_waitcnt vmcnt(11)\n"
    "v_mfma_f32_32x32x16_bf16 v[216:231], v[100:103], v[204:207], v[216:231]\n"
    "v_exp_f32 v188, v188\n"
    "v_exp_f32 v189, v189\n"
    "v_exp_f32 v190, v190\n"
    "v_exp_f32 v191, v191\n"
    "ds_write_b128 v254, v[232:235] offset:0\n"
    "s_add_u32 s52, s30, 0x3000\n"
    "buffer_load_dwordx4 a[56:59], v1, s[8:11], s52 offen offset:0 sc1\n"
    "v_cvt_pk_bf16_f32 v200, v184, v185\n"
    "v_cvt_pk_bf16_f32 v201, v186, v187\n"
    "v_mfma_f32_32x32x16_bf16 v[168:183], v[76:79], v[140:143], v[168:183]\n"
    "v_cvt_pk_bf16_f32 v202, v188, v189\n"
    "v_cvt_pk_bf16_f32 v203, v190, v191\n"
    "v_exp_f32 v192, v192\n"
    "v_exp_f32 v193, v193\n"
    "v_exp_f32 v194, v194\n"
    "v_exp_f32 v195, v195\n"
    "v_mfma_f32_32x32x16_bf16 v[216:231], v[104:107], v[200:203], v[216:231]\n"
    "v_exp_f32 v196, v196\n"
    "v_exp_f32 v197, v197\n"
    "v_exp_f32 v198, v198\n"
    "v_exp_f32 v199, v199\n"
    "ds_write_b128 v254, v[236:239] offset:1024\n"
    "buffer_load_dwordx4 a[60:63], v1, s[8:11], s52 offen offset:1024 sc1\n"
    "v_cvt_pk_bf16_f32 v204, v192, v193\n"
    "v_cvt_pk_bf16_f32 v205, v194, v195\n"
    "v_cvt_pk_bf16_f32 v206, v196, v197\n"
    "v_cvt_pk_bf16_f32 v207, v198, v199\n"
    "v_mfma_f32_32x32x16_bf16 v[184:199], v[80:83], v[136:139], 0\n"
    "v_exp_f32 v168, v168\n"
    "v_exp_f32 v169, v169\n"
    "v_exp_f32 v170, v170\n"
    "v_exp_f32 v171, v171\n"
    "v_mfma_f32_32x32x16_bf16 v[216:231], v[108:111], v[204:207], v[216:231]\n"
    "v_exp_f32 v172, v172\n"
    "v_exp_f32 v173, v173\n"
    "v_exp_f32 v174, v174\n"
    "v_exp_f32 v175, v175\n"
    "ds_write_b128 v254, v[240:243] offset:2048\n"
    "buffer_load_dwordx4 a[64:67], v1, s[8:11], s52 offen offset:2048 sc1\n"
    "v_cvt_pk_bf16_f32 v200, v168, v169\n"
    "v_cvt_pk_bf16_f32 v201, v170, v171\n"
    "v_mfma_f32_32x32x16_bf16 v[184:199], v[84:87], v[140:143], v[184:199]\n"
    "v_cvt_pk_bf16_f32 v202, v172, v173\n"
    "v_cvt_pk_bf16_f32 v203, v174, v175\n"
    "v_exp_f32 v176, v176\n"
    "v_exp_f32 v177, v177\n"
    "v_exp_f32 v178, v178\n"
    "v_exp_f32 v179, v179\n"
    "v_mfma_f32_32x32x16_bf16 v[216:231], v[112:115], v[200:203], v[216:231]\n"
    "v_exp_f32 v180, v180\n"
    "v_exp_f32 v181, v181\n"
    "v_exp_f32 v182, v182\n"
    "v_exp_f32 v183, v183\n"
    "buffer_load_dwordx4 a[68:71], v1, s[8:11], s52 offen offset:3072 sc1\n"
    "v_cvt_pk_bf16_f32 v204, v176, v177\n"
    "v_cvt_pk_bf16_f32 v205, v178, v179\n"
    "v_cvt_pk_bf16_f32 v206, v180, v181\n"
    "v_cvt_pk_bf16_f32 v207, v182, v183\n"
    "v_mfma_f32_32x32x16_bf16 v[168:183], v[88:91], v[136:139], 0\n"
    "v_exp_f32 v184, v184\n"
    "v_exp_f32 v185, v185\n"
    "v_exp_f32 v186, v186\n"
    "v_exp_f32 v187, v187\n"
    "v_mfma_f32_32x32x16_bf16 v[216:231], v[116:119], v[204:207], v[216:231]\n"
    "v_exp_f32 v188, v188\n"
    "v_exp_f32 v189, v189\n"
    "v_exp_f32 v190, v190\n"
    "v_exp_f32 v191, v191\n"
    "s_waitcnt lgkmcnt(0)\n"
    "s_barrier\n"
    "ds_read_b32 v72, v55 offset:4096\n"
    "ds_read_b128 v[56:59], v55 offset:0\n"
    "ds_read_b128 v[60:63], v55 offset:1024\n"
    "ds_read_b128 v[64:67], v55 offset:2048\n"
    "ds_read_b128 v[68:71], v55 offset:3072\n"
    "v_cvt_pk_bf16_f32 v200, v184, v185\n"
    "v_cvt_pk_bf16_f32 v201, v186, v187\n"
    "v_mfma_f32_32x32x16_bf16 v[168:183], v[92:95], v[140:143], v[168:183]\n"
    "v_cvt_pk_bf16_f32 v202, v188, v189\n"
    "v_cvt_pk_bf16_f32 v203, v190, v191\n"
    "v_exp_f32 v192, v192\n"
    "v_exp_f32 v193, v193\n"
    "v_exp_f32 v194, v194\n"
    "v_exp_f32 v195, v195\n"
    "v_mfma_f32_32x32x16_bf16 v[216:231], v[120:123], v[200:203], v[216:231]\n"
    "v_exp_f32 v196, v196\n"
    "v_exp_f32 v197, v197\n"
    "v_exp_f32 v198, v198\n"
    "v_exp_f32 v199, v199\n"
    "v_cvt_pk_bf16_f32 v204, v192, v193\n"
    "v_cvt_pk_bf16_f32 v205, v194, v195\n"
    "v_cvt_pk_bf16_f32 v206, v196, v197\n"
    "v_cvt_pk_bf16_f32 v207, v198, v199\n"
    "v_exp_f32 v168, v168\n"
    "v_exp_f32 v169, v169\n"
    "v_exp_f32 v170, v170\n"
    "v_exp_f32 v171, v171\n"
    "v_mfma_f32_32x32x16_bf16 v[216:231], v[124:127], v[204:207], v[216:231]\n"
    "v_exp_f32 v172, v172\n"
    "v_exp_f32 v173, v173\n"
    "v_exp_f32 v174, v174\n"
    "v_exp_f32 v175, v175\n"
    "s_waitcnt lgkmcnt(4)\n"
    "v_max_f32 v73, v248, v72\n"
    "v_sub_f32 v74, v248, v73\n"
    "v_sub_f32 v75, v72, v73\n"
    "v_exp_f32 v74, v74\n"
    "v_exp_f32 v75, v75\n"
    "s_waitcnt lgkmcnt(0)\n"
    "v_mul_f32 v76, v75, v68\n"
    "v_cvt_pk_bf16_f32 v200, v168, v169\n"
    "v_cvt_pk_bf16_f32 v201, v170, v171\n"
    "v_cvt_pk_bf16_f32 v202, v172, v173\n"
    "v_cvt_pk_bf16_f32 v203, v174, v175\n"
    "v_exp_f32 v176, v176\n"
    "v_exp_f32 v177, v177\n"
    "v_exp_f32 v178, v178\n"
    "v_exp_f32 v179, v179\n"
    "v_mfma_f32_32x32x16_bf16 v[216:231], v[128:131], v[200:203], v[216:231]\n"
    "v_exp_f32 v180, v180\n"
    "v_exp_f32 v181, v181\n"
    "v_exp_f32 v182, v182\n"
    "v_exp_f32 v183, v183\n"
    "v_cvt_pk_bf16_f32 v204, v176, v177\n"
    "v_cvt_pk_bf16_f32 v205, v178, v179\n"
    "v_cvt_pk_bf16_f32 v206, v180, v181\n"
    "v_cvt_pk_bf16_f32 v207, v182, v183\n"
    "s_nop 1\n"
    "v_mfma_f32_32x32x16_bf16 v[216:231], v[132:135], v[204:207], v[216:231]\n"
    "; merge with the partner wave; denominator: row 28 = register 12 of the upper half\n"
    "s_nop 11\n"
    "v_fmac_f32 v76, v74, v228\n"
    "v_mov_b32 v77, v76\n"
    "s_nop 1\n"
    "v_permlane32_swap_b32 v76, v77\n"
    "v_rcp_f32 v77, v77\n"
    "s_nop 0\n"
    "v_mul_f32 v74, v74, v77\n"
    "v_mul_f32 v75, v75, v77\n"
    "v_pk_mul_f32 v[216:217], v[74:75], v[216:217] op_sel_hi:[0,1]\n"
    "v_pk_mul_f32 v[218:219], v[74:75], v[218:219] op_sel_hi:[0,1]\n"
    "v_pk_mul_f32 v[220:221], v[74:75], v[220:221] op_sel_hi:[0,1]\n"
    "v_pk_mul_f32 v[222:223], v[74:75], v[222:223] op_sel_hi:[0,1]\n"
    "v_pk_fma_f32 v[216:217], v[74:75], v[56:57], v[216:217] op_sel:[1,0,0]\n"
    "v_pk_fma_f32 v[218:219], v[74:75], v[58:59], v[218:219] op_sel:[1,0,0]\n"
    "v_pk_fma_f32 v[220:221], v[74:75], v[60:61], v[220:221] op_sel:[1,0,0]\n"
    "v_pk_fma_f32 v[222:223], v[74:75], v[62:63], v[222:223] op_sel:[1,0,0]\n"
    "v_cvt_pk_bf16_f32 v200, v216, v217\n"
    "v_cvt_pk_bf16_f32 v201, v218, v219\n"
    "v_cvt_pk_bf16_f32 v202, v220, v221\n"
    "v_cvt_pk_bf16_f32 v203, v222, v223\n"
    "buffer_store_dwordx4 v[200:203], v255, s[8:11], s72 offen offset:0 sc1\n"
    "v_pk_mul_f32 v[224:225], v[74:75], v[224:225] op_sel_hi:[0,1]\n"
    "v_pk_mul_f32 v[226:227], v[74:75], v[226:227] op_sel_hi:[0,1]\n"
    "v_pk_mul_f32 v[228:229], v[74:75], v[228:229] op_sel_hi:[0,1]\n"
    "v_pk_mul_f32 v[230:231], v[74:75], v[230:231] op_sel_hi:[0,1]\n"
    "v_pk_fma_f32 v[224:225], v[74:75], v[64:65], v[224:225] op_sel:[1,0,0]\n"
    "v_pk_fma_f32 v[226:227], v[74:75], v[66:67], v[226:227] op_sel:[1,0,0]\n"
    "v_pk_fma_f32 v[228:229], v[74:75], v[68:69], v[228:229] op_sel:[1,0,0]\n"
    "v_pk_fma_f32 v[230:231], v[74:75], v[70:71], v[230:231] op_sel:[1,0,0]\n"
    "v_cvt_pk_bf16_f32 v204, v224, v225\n"
    "v_cvt_pk_bf16_f32 v205, v226, v227\n"
    "v_cvt_pk_bf16_f32 v206, v228, v229\n"
    "v_cvt_pk_bf16_f32 v207, v230, v231\n"
    "buffer_store_dwordx4 v[204:207], v255, s[8:11], s72 offen offset:512 sc1\n"
    "; signal 1: every wave has its write-through stores acknowledged; barrier; one wave stores the flag line\n"
    "s_add_u32 s52, s30, 0x4000\n"
    "buffer_load_dwordx4 a[72:75], v1, s[8:11], s52 offen offset:0 sc1\n"
    "buffer_load_dwordx4 a[76:79], v1, s[8:11], s52 offen offset:1024 sc1\n"
    "buffer_load_dwordx4 a[80:83], v1, s[8:11], s52 offen offset:2048 sc1\n"
    "buffer_load_dwordx4 a[84:87], v1, s[8:11], s52 offen offset:3072 sc1\n"
    "s_add_u32 s52, s30, 0x5000\n"
    "buffer_load_dwordx4 a[88:91], v1, s[8:11], s52 offen offset:0 sc1\n"
    "buffer_load_dwordx4 a[92:95], v1, s[8:11], s52 offen offset:1024 sc1\n"
    "buffer_load_dwordx4 a[96:99], v1, s[8:11], s52 offen offset:2048 sc1\n"
    "buffer_load_dwordx4 a[100:103], v1, s[8:11], s52 offen offset:3072 sc1\n"
    "s_add_u32 s52, s30, 0x6000\n"
    "buffer_load_dwordx4 a[104:107], v1, s[8:11], s52 offen offset:0 sc1\n"
    "s_waitcnt vmcnt(9)\n"
    "s_barrier\n"
    "s_cmp_lg_u32 s20, 0\n"
    "s_cbranch_scc1 Lskip_27_%=\n"
    "s_add_u32 s60, s39, 0x8000\n"
    "s_add_u32 s60, s60, s38\n"
    "s_mov_b64 exec, 0xff\n"
    "buffer_store_dwordx4 v[36:39], v1, s[8:11], s60 offen sc1\n"
    "s_mov_b64 exec, -1\n"
    "Lskip_27_%=:\n"
    "s_sleep 1\n"
    "buffer_load_dwordx4 a[108:111], v1, s[8:11], s52 offen offset:1024 sc1\n"
    "buffer_load_dwordx4 a[112:115], v1, s[8:11], s52 offen offset:2048 sc1\n"
    "s_sleep 1\n"
    "buffer_load_dwordx4 a[116:119], v1, s[8:11], s52 offen offset:3072 sc1\n"
    "s_sleep 1\n"
    "s_add_u32 s52, s30, 0x7000\n"
    "buffer_load_dwordx4 a[0:3], v45, s[8:11], s52 offen offset:0 sc1\n"
    "s_sleep 1\n"
    "buffer_load_dwordx4 a[4:7], v45, s[8:11], s52 offen offset:1024 sc1\n"
    "s_sleep 1\n"
    "buffer_load_dwordx4 a[8:11], v45, s[8:11], s52 offen offset:2048 sc1\n"
    "s_sleep 1\n"
    "buffer_load_dwordx4 a[12:15], v45, s[8:11], s52 offen offset:3072 sc1\n"
    "s_sleep 1\n"
    "s_add_u32 s52, s30, 0x8000\n"
    "buffer_load_dwordx4 a[16:19], v45, s[8:11], s52 offen offset:0 sc1\n"
    "buffer_load_dwordx4 a[20:23], v45, s[8:11], s52 offen offset:1024 sc1\n"
    "s_sleep 1\n"
    "s_sleep 1\n"
    "buffer_load_dwordx4 a[24:27], v45, s[8:11], s52 offen offset:2048 sc1\n"
    "buffer_load_dwordx4 a[28:31], v45, s[8:11], s52 offen offset:3072 sc1\n"
    "s_sleep 1\n"
    "s_add_u32 s52, s30, 0x9000\n"
    "buffer_load_dwordx4 a[120:123], v45, s[8:11], s52 offen offset:0 sc1\n"
    "s_sleep 1\n"
    "buffer_load_dwordx4 a[124:127], v45, s[8:11], s52 offen offset:1024 sc1\n"
    "s_add_u32 s71, s38, 0x8000\n"
    "buffer_load_dwordx2 v[46:47], v5, s[8:11], s71 offen sc1\n"
    "buffer_load_dwordx4 a[128:131], v45, s[8:11], s52 offen offset:2048 sc1\n"
    "s_cmp_eq_u32 s22, 7\n"
    "s_cbranch_scc1 Lskip_28_%=\n"
    "s_lshl_b32 s55, s20, 14\n"
    "s_add_u32 s65, s55, s36\n"
    "s_waitcnt vmcnt(1)\n"
    "v_cmp_ne_u64 vcc, s[26:27], v[46:47]\n"
    "s_nop 0\n"
    "s_cbranch_vccnz Lspin_29_%=\n"
    "Lgo_30_%=:\n"
    "; WO\n"
    "s_add_u32 s53, s65, 0x0\n"
    "s_add_u32 m0, s25, 0x0\n"
    "s_nop 0\n"
    "buffer_load_dwordx4 v1, s[8:11], s53 offen offset:0 sc1 lds\n"
    "buffer_load_dwordx4 v1, s[8:11], s53 offen offset:1024 sc1 lds\n"
    "buffer_load_dwordx4 v1, s[8:11], s53 offen offset:2048 sc1 lds\n"
    "buffer_load_dwordx4 v1, s[8:11], s53 offen offset:3072 sc1 lds\n"
    "s_add_u32 s53, s65, 0x1000\n"
    "s_add_u32 m0, s25, 0x1000\n"
    "s_nop 0\n"
    "buffer_load_dwordx4 v1, s[8:11], s53 offen offset:0 sc1 lds\n"
    "buffer_load_dwordx4 v1, s[8:11], s53 offen offset:1024 sc1 lds\n"
    "buffer_load_dwordx4 v1, s[8:11], s53 offen offset:2048 sc1 lds\n"
    "buffer_load_dwordx4 v1, s[8:11], s53 offen offset:3072 sc1 lds\n"
    "s_add_u32 s53, s65, 0x2000\n"
    "s_add_u32 m0, s25, 0x2000\n"
    "s_nop 0\n"
    "buffer_load_dwordx4 v1, s[8:11], s53 offen offset:0 sc1 lds\n"
    "buffer_load_dwordx4 v1, s[8:11], s53 offen offset:1024 sc1 lds\n"
    "s_waitcnt vmcnt(8)\n"
    "s_barrier\n"
    "ds_read_b128 v[56:59], v6 offset:0\n"
    "ds_read_b128 v[60:63], v6 offset:20480\n"
    "ds_read_b128 v[64:67], v7 offset:0\n"
    "ds_read_b128 v[68:71], v7 offset:20480\n"
    "buffer_load_dwordx4 v1, s[8:11], s53 offen offset:2048 sc1 lds\n"
    "s_waitcnt lgkmcnt(3)\n"
    "v_mfma_f32_16x16x32_bf16 v[118:121], a[168:171], v[56:59], 0\n"
    "ds_read_b128 v[72:75], v6 offset:1024\n"
    "buffer_load_dwordx4 v1, s[8:11], s53 offen offset:3072 sc1 lds\n"
    "s_waitcnt lgkmcnt(3)\n"
    "v_mfma_f32_16x16x32_bf16 v[122:125], a[168:171], v[60:63], 0\n"
    "ds_read_b128 v[76:79], v6 offset:21504\n"
    "s_waitcnt lgkmcnt(3)\n"
    "v_mfma_f32_16x16x32_bf16 v[126:129], a[168:171], v[64:67], 0\n"
    "ds_read_b128 v[80:83], v7 offset:1024\n"
    "s_waitcnt lgkmcnt(3)\n"
    "v_mfma_f32_16x16x32_bf16 v[130:133], a[168:171], v[68:71], 0\n"
    "ds_read_b128 v[84:87], v7 offset:21504\n"
    "s_add_u32 s53, s65, 0x3000\n"
    "s_add_u32 m0, s25, 0x3000\n"
    "s_nop 0\n"
    "buffer_load_dwordx4 v1, s[8:11], s53 offen offset:0 sc1 lds\n"
    "s_waitcnt vmcnt(8)\n"
    "s_barrier\n"
    "s_waitcnt lgkmcnt(3)\n"
    "v_mfma_f32_16x16x32_bf16 v[118:121], a[172:175], v[72:75], v[118:121]\n"
    "ds_read_b128 v[56:59], v6 offset:2048\n"
    "buffer_load_dwordx4 v1, s[8:11], s53 offen offset:1024 sc1 lds\n"
    "s_waitcnt lgkmcnt(3)\n"
    "v_mfma_f32_16x16x32_bf16 v[122:125], a[172:175], v[76:79], v[122:125]\n"
    "ds_read_b128 v[60:63], v6 offset:22528\n"
    "buffer_load_dwordx4 v1, s[8:11], s53 offen offset:2048 sc1 lds\n"
    "s_waitcnt lgkmcnt(3)\n"
    "v_mfma_f32_16x16x32_bf16 v[126:129], a[172:175], v[80:83], v[126:129]\n"
    "ds_read_b128 v[64:67], v7 offset:2048\n"
    "s_waitcnt lgkmcnt(3)\n"
    "v_mfma_f32_16x16x32_bf16 v[130:133], a[172:175], v[84:87], v[130:133]\n"
    "ds_read_b128 v[68:71], v7 offset:22528\n"
    "s_waitcnt lgkmcnt(3)\n"
    "v_mfma_f32_16x16x32_bf16 v[118:121], a[176:179], v[56:59], v[118:121]\n"
    "ds_read_b128 v[72:75], v6 offset:3072\n"
    "s_waitcnt lgkmcnt(3)\n"
    "v_mfma_f32_16x16x32_bf16 v[122:125], a[176:179], v[60:63], v[122:125]\n"
    "ds_read_b128 v[76:79], v6 offset:23552\n"
    "buffer_load_dwordx4 v1, s[8:11], s53 offen offset:3072 sc1 lds\n"
    "s_waitcnt lgkmcnt(3)\n"
    "v_mfma_f32_16x16x32_bf16 v[126:129], a[176:179], v[64:67], v[126:129]\n"
    "ds_read_b128 v[80:83], v7 offset:3072\n"
    "s_waitcnt lgkmcnt(3)\n"
    "v_mfma_f32_16x16x32_bf16 v[130:133], a[176:179], v[68:71], v[130:133]\n"
    "ds_read_b128 v[84:87], v7 offset:23552\n"
    "s_waitcnt lgkmcnt(3)\n"
    "v_mfma_f32_16x16x32_bf16 v[118:121], a[180:183], v[72:75], v[118:121]\n"
    "ds_read_b128 v[56:59], v6 offset:4096\n"
    "buffer_load_dwordx4 v[214:217], v41, s[8:11], s31 offen offset:0 sc1\n"
    "s_waitcnt lgkmcnt(3)\n"
    "v_mfma_f32_16x16x32_bf16 v[122:125], a[180:183], v[76:79], v[122:125]\n"
    "ds_read_b128 v[60:63], v6 offset:24576\n"
    "s_waitcnt lgkmcnt(3)\n"
    "v_mfma_f32_16x16x32_bf16 v[126:129], a[180:183], v[80:83], v[126:129]\n"
    "ds_read_b128 v[64:67], v7 offset:4096\n"
    "s_waitcnt lgkmcnt(3)\n"
    "v_mfma_f32_16x16x32_bf16 v[130:133], a[180:183], v[84:87], v[130:133]\n"
    "ds_read_b128 v[68:71], v7 offset:24576\n"
    "s_waitcnt vmcnt(9)\n"
    "s_barrier\n"
    "s_waitcnt lgkmcnt(3)\n"
    "v_mfma_f32_16x16x32_bf16 v[118:121], a[184:187], v[56:59], v[118:121]\n"
    "ds_read_b128 v[72:75], v6 offset:5120\n"
    "buffer_load_dwordx4 v[218:221], v41, s[8:11], s31 offen offset:1024 sc1\n"
    "s_waitcnt lgkmcnt(3)\n"
    "v_mfma_f32_16x16x32_bf16 v[122:125], a[184:187], v[60:63], v[122:125]\n"
    "ds_read_b128 v[76:79], v6 offset:25600\n"
    "buffer_load_dwordx4 v[222:225], v41, s[8:11], s31 offen offset:2048 sc1\n"
    "s_waitcnt lgkmcnt(3)\n"
    "v_mfma_f32_16x16x32_bf16 v[126:129], a[184:187], v[64:67], v[126:129]\n"
    "ds_read_b128 v[80:83], v7 offset:5120\n"
    "s_waitcnt lgkmcnt(3)\n"
    "v_mfma_f32_16x16x32_bf16 v[130:133], a[184:187], v[68:71], v[130:133]\n"
    "ds_read_b128 v[84:87], v7 offset:25600\n"
    "s_waitcnt lgkmcnt(3)\n"
    "v_mfma_f32_16x16x32_bf16 v[118:121], a[188:191], v[72:75], v[118:121]\n"
    "ds_read_b128 v[56:59], v6 offset:6144\n"
    "s_waitcnt lgkmcnt(3)\n"
    "v_mfma_f32_16x16x32_bf16 v[122:125], a[188:191], v[76:79], v[122:125]\n"
    "ds_read_b128 v[60:63], v6 offset:26624\n"
    "s_waitcnt lgkmcnt(3)\n"
    "v_mfma_f32_16x16x32_bf16 v[126:129], a[188:191], v[80:83], v[126:129]\n"
    "ds_read_b128 v[64:67], v7 offset:6144\n"
    "buffer_load_dwordx4 v[226:229], v41, s[8:11], s31 offen offset:3072 sc1\n"
    "s_waitcnt lgkmcnt(3)\n"
    "v_mfma_f32_16x16x32_bf16 v[130:133], a[188:191], v[84:87], v[130:133]\n"
    "ds_read_b128 v[68:71], v7 offset:26624\n"
    "s_waitcnt lgkmcnt(3)\n"
    "v_mfma_f32_16x16x32_bf16 v[118:121], a[192:195], v[56:59], v[118:121]\n"
    "ds_read_b128 v[72:75], v6 offset:7168\n"
    "s_waitcnt lgkmcnt(3)\n"
    "v_mfma_f32_16x16x32_bf16 v[122:125], a[192:195], v[60:63], v[122:125]\n"
    "ds_read_b128 v[76:79], v6 offset:27648\n"
    "s_waitcnt lgkmcnt(3)\n"
    "v_mfma_f32_16x16x32_bf16 v[126:129], a[192:195], v[64:67], v[126:129]\n"
    "ds_read_b128 v[80:83], v7 offset:7168\n"
    "s_add_u32 s52, s31, 0x1000\n"
    "buffer_load_dwordx4 v[230:233], v41, s[8:11], s52 offen offset:0 sc1\n"
    "s_waitcnt lgkmcnt(3)\n"
    "v_mfma_f32_16x16x32_bf16 v[130:133], a[192:195], v[68:71], v[130:133]\n"
    "ds_read_b128 v[84:87], v7 offset:27648\n"
    "s_waitcnt vmcnt(10)\n"
    "s_barrier\n"
    "s_waitcnt lgkmcnt(3)\n"
    "v_mfma_f32_16x16x32_bf16 v[118:121], a[196:199], v[72:75], v[118:121]\n"
    "ds_read_b128 v[56:59], v6 offset:8192\n"
    "buffer_load_dwordx4 v[234:237], v41, s[8:11], s52 offen offset:1024 sc1\n"
    "s_waitcnt lgkmcnt(3)\n"
    "v_mfma_f32_16x16x32_bf16 v[122:125], a[196:199], v[76:79], v[122:125]\n"
    "ds_read_b128 v[60:63], v6 offset:28672\n"
    "s_waitcnt lgkmcnt(3)\n"
    "v_mfma_f32_16x16x32_bf16 v[126:129], a[196:199], v[80:83], v[126:129]\n"
    "ds_read_b128 v[64:67], v7 offset:8192\n"
    "s_waitcnt lgkmcnt(3)\n"
    "v_mfma_f32_16x16x32_bf16 v[130:133], a[196:199], v[84:87], v[130:133]\n"
    "ds_read_b128 v[68:71], v7 offset:28672\n"
    "buffer_load_dwordx4 v[238:241], v41, s[8:11], s52 offen offset:2048 sc1\n"
    "s_waitcnt lgkmcnt(3)\n"
    "v_mfma_f32_16x16x32_bf16 v[118:121], a[200:203], v[56:59], v[118:121]\n"
    "ds_read_b128 v[72:75], v6 offset:9216\n"
    "s_waitcnt lgkmcnt(3)\n"
    "v_mfma_f32_16x16x32_bf16 v[122:125], a[200:203], v[60:63], v[122:125]\n"
    "ds_read_b128 v[76:79], v6 offset:29696\n"
    "s_waitcnt lgkmcnt(3)\n"
    "v_mfma_f32_16x16x32_bf16 v[126:129], a[200:203], v[64:67], v[126:129]\n"
    "ds_read_b128 v[80:83], v7 offset:9216\n"
    "buffer_load_dwordx4 v[242:245], v41, s[8:11], s52 offen offset:3072 sc1\n"
    "s_waitcnt lgkmcnt(3)\n"
    "v_mfma_f32_16x16x32_bf16 v[130:133], a[200:203], v[68:71], v[130:133]\n"
    "ds_read_b128 v[84:87], v7 offset:29696\n"
    "s_waitcnt lgkmcnt(3)\n"
    "v_mfma_f32_16x16x32_bf16 v[118:121], a[204:207], v[72:75], v[118:121]\n"
    "ds_read_b128 v[56:59], v6 offset:10240\n"
    "s_waitcnt lgkmcnt(3)\n"
    "v_mfma_f32_16x16x32_bf16 v[122:125], a[204:207], v[76:79], v[122:125]\n"
    "ds_read_b128 v[60:63], v6 offset:30720\n"
    "s_waitcnt lgkmcnt(3)\n"
    "v_mfma_f32_16x16x32_bf16 v[126:129], a[204:207], v[80:83], v[126:129]\n"
    "ds_read_b128 v[64:67], v7 offset:10240\n"
    "s_add_u32 s52, s31, 0x2000\n"
    "buffer_load_dwordx4 v[246:249], v41, s[8:11], s52 offen offset:0 sc1\n"
    "s_waitcnt lgkmcnt(3)\n"
    "v_mfma_f32_16x16x32_bf16 v[130:133], a[204:207], v[84:87], v[130:133]\n"
    "ds_read_b128 v[68:71], v7 offset:30720\n"
    "s_waitcnt vmcnt(11)\n"
    "s_barrier\n"
    "s_waitcnt lgkmcnt(3)\n"
    "v_mfma_f32_16x16x32_bf16 v[118:121], a[208:211], v[56:59], v[118:121]\n"
    "ds_read_b128 v[72:75], v6 offset:11264\n"
    "buffer_load_dwordx4 v[250:253], v41, s[8:11], s52 offen offset:1024 sc1\n"
    "s_waitcnt lgkmcnt(3)\n"
    "v_mfma_f32_16x16x32_bf16 v[122:125], a[208:211], v[60:63], v[122:125]\n"
    "ds_read_b128 v[76:79], v6 offset:31744\n"
    "s_waitcnt lgkmcnt(3)\n"
    "v_mfma_f32_16x16x32_bf16 v[126:129], a[208:211], v[64:67], v[126:129]\n"
    "ds_read_b128 v[80:83], v7 offset:11264\n"
    "s_waitcnt lgkmcnt(3)\n"
    "v_mfma_f32_16x16x32_bf16 v[130:133], a[208:211], v[68:71], v[130:133]\n"
    "ds_read_b128 v[84:87], v7 offset:31744\n"
    "buffer_load_dwordx4 a[200:203], v41, s[8:11], s52 offen offset:2048 sc1\n"
    "s_waitcnt lgkmcnt(3)\n"
    "v_mfma_f32_16x16x32_bf16 v[118:121], a[212:215], v[72:75], v[118:121]\n"
    "ds_read_b128 v[56:59], v6 offset:12288\n"
    "s_waitcnt lgkmcnt(3)\n"
    "v_mfma_f32_16x16x32_bf16 v[122:125], a[212:215], v[76:79], v[122:125]\n"
    "ds_read_b128 v[60:63], v6 offset:32768\n"
    "s_waitcnt lgkmcnt(3)\n"
    "v_mfma_f32_16x16x32_bf16 v[126:129], a[212:215], v[80:83], v[126:129]\n"
    "ds_read_b128 v[64:67], v7 offset:12288\n"
    "buffer_load_dwordx4 a[204:207], v41, s[8:11], s52 offen offset:3072 sc1\n"
    "s_waitcnt lgkmcnt(3)\n"
    "v_mfma_f32_16x16x32_bf16 v[130:133], a[212:215], v[84:87], v[130:133]\n"
    "ds_read_b128 v[68:71], v7 offset:32768\n"
    "s_waitcnt lgkmcnt(3)\n"
    "v_mfma_f32_16x16x32_bf16 v[118:121], a[216:219], v[56:59], v[118:121]\n"
    "ds_read_b128 v[72:75], v6 offset:13312\n"
    "s_waitcnt lgkmcnt(3)\n"
    "v_mfma_f32_16x16x32_bf16 v[122:125], a[216:219], v[60:63], v[122:125]\n"
    "ds_read_b128 v[76:79], v6 offset:33792\n"
    "s_waitcnt lgkmcnt(3)\n"
    "v_mfma_f32_16x16x32_bf16 v[126:129], a[216:219], v[64:67], v[126:129]\n"
    "ds_read_b128 v[80:83], v7 offset:13312\n"
    "s_waitcnt lgkmcnt(3)\n"
    "v_mfma_f32_16x16x32_bf16 v[130:133], a[216:219], v[68:71], v[130:133]\n"
    "ds_read_b128 v[84:87], v7 offset:33792\n"
    "s_waitcnt vmcnt(12)\n"
    "s_barrier\n"
    "s_waitcnt lgkmcnt(3)\n"
    "v_mfma_f32_16x16x32_bf16 v[118:121], a[220:223], v[72:75], v[118:121]\n"
    "ds_read_b128 v[56:59], v6 offset:14336\n"
    "s_waitcnt lgkmcnt(3)\n"
    "v_mfma_f32_16x16x32_bf16 v[122:125], a[220:223], v[76:79], v[122:125]\n"
    "ds_read_b128 v[60:63], v6 offset:34816\n"
    "s_waitcnt lgkmcnt(3)\n"
    "v_mfma_f32_16x16x32_bf16 v[126:129], a[220:223], v[80:83], v[126:129]\n"
    "ds_read_b128 v[64:67], v7 offset:14336\n"
    "s_waitcnt lgkmcnt(3)\n"
    "v_mfma_f32_16x16x32_bf16 v[130:133], a[220:223], v[84:87], v[130:133]\n"
    "ds_read_b128 v[68:71], v7 offset:34816\n"
    "s_waitcnt lgkmcnt(3)\n"
    "v_mfma_f32_16x16x32_bf16 v[118:121], a[224:227], v[56:59], v[118:121]\n"
    "ds_read_b128 v[72:75], v6 offset:15360\n"
    "s_waitcnt lgkmcnt(3)\n"
    "v_mfma_f32_16x16x32_bf16 v[122:125], a[224:227], v[60:63], v[122:125]\n"
    "ds_read_b128 v[76:79], v6 offset:35840\n"
    "s_waitcnt lgkmcnt(3)\n"
    "v_mfma_f32_16x16x32_bf16 v[126:129], a[224:227], v[64:67], v[126:129]\n"
    "ds_read_b128 v[80:83], v7 offset:15360\n"
    "s_waitcnt lgkmcnt(3)\n"
    "v_mfma_f32_16x16x32_bf16 v[130:133], a[224:227], v[68:71], v[130:133]\n"
    "ds_read_b128 v[84:87], v7 offset:35840\n"
    "s_waitcnt lgkmcnt(3)\n"
    "v_mfma_f32_16x16x32_bf16 v[118:121], a[228:231], v[72:75], v[118:121]\n"
    "s_waitcnt lgkmcnt(2)\n"
    "v_mfma_f32_16x16x32_bf16 v[122:125], a[228:231], v[76:79], v[122:125]\n"
    "s_waitcnt lgkmcnt(1)\n"
    "v_mfma_f32_16x16x32_bf16 v[126:129], a[228:231], v[80:83], v[126:129]\n"
    "s_waitcnt lgkmcnt(0)\n"
    "v_mfma_f32_16x16x32_bf16 v[130:133], a[228:231], v[84:87], v[130:133]\n"
    "s_nop 1\n"
    "v_pk_add_f32 v[8:9], v[118:119], v[8:9]\n"
    "v_pk_add_f32 v[10:11], v[120:121], v[10:11]\n"
    "v_pk_add_f32 v[12:13], v[122:123], v[12:13]\n"
    "v_pk_add_f32 v[14:15], v[124:125], v[14:15]\n"
    "v_pk_add_f32 v[16:17], v[126:127], v[16:17]\n"
    "v_pk_add_f32 v[18:19], v[128:129], v[18:19]\n"
    "v_pk_add_f32 v[20:21], v[130:131], v[20:21]\n"
    "v_pk_add_f32 v[22:23], v[132:133], v[22:23]\n"
    "v_cvt_pk_bf16_f32 v134, v8, v9\n"
    "v_cvt_pk_bf16_f32 v135, v10, v11\n"
    "v_cvt_pk_bf16_f32 v136, v12, v13\n"
    "v_cvt_pk_bf16_f32 v137, v14, v15\n"
    "v_cvt_pk_bf16_f32 v138, v16, v17\n"
    "v_cvt_pk_bf16_f32 v139, v18, v19\n"
    "v_cvt_pk_bf16_f32 v140, v20, v21\n"
    "v_cvt_pk_bf16_f32 v141, v22, v23\n"
    "v_dot2_f32_bf16 v142, s42, v134, 0\n"
    "v_dot2_f32_bf16 v146, v134, v134, 0\n"
    "v_dot2_f32_bf16 v143, s42, v136, 0\n"
    "v_dot2_f32_bf16 v147, v136, v136, 0\n"
    "v_dot2_f32_bf16 v144, s42, v138, 0\n"
    "v_dot2_f32_bf16 v148, v138, v138, 0\n"
    "v_dot2_f32_bf16 v145, s42, v140, 0\n"
    "v_dot2_f32_bf16 v149, v140, v140, 0\n"
    "v_dot2_f32_bf16 v142, s42, v135, v142\n"
    "v_dot2_f32_bf16 v146, v135, v135, v146\n"
    "v_dot2_f32_bf16 v143, s42, v137, v143\n"
    "v_dot2_f32_bf16 v147, v137, v137, v147\n"
    "v_dot2_f32_bf16 v144, s42, v139, v144\n"
    "v_dot2_f32_bf16 v148, v139, v139, v148\n"
    "v_dot2_f32_bf16 v145, s42, v141, v145\n"
    "v_dot2_f32_bf16 v149, v141, v141, v149\n"
    "s_add_u32 s54, s75, s40\n"
    "s_add_u32 s55, s54, 0x7000\n"
    "s_add_u32 s56, s75, s41\n"
    "v_permlane16_swap_b32 v134, v136\n"
    "v_permlane16_swap_b32 v135, v137\n"
    "buffer_store_dwordx4 v[134:137], v2, s[8:11], s54 offen sc1\n"
    "v_permlane16_swap_b32 v138, v140\n"
    "v_permlane16_swap_b32 v139, v141\n"
    "buffer_store_dwordx4 v[138:141], v2, s[8:11], s55 offen sc1\n"
    "v_permlane16_swap_b32 v142, v143\n"
    "v_permlane16_swap_b32 v144, v145\n"
    "v_permlane16_swap_b32 v146, v147\n"
    "v_permlane16_swap_b32 v148, v149\n"
    "v_add_f32 v142, v142, v143\n"
    "v_add_f32 v144, v144, v145\n"
    "v_add_f32 v146, v146, v147\n"
    "v_add_f32 v148, v148, v149\n"
    "v_permlane32_swap_b32 v142, v144\n"
    "s_nop 0\n"
    "v_permlane32_swap_b32 v146, v148\n"
    "v_add_f32 v142, v142, v144\n"
    "v_add_f32 v143, v146, v148\n"
    "buffer_store_dwordx2 v[142:143], v3, s[8:11], s56 offen sc1\n"
    "Lskip_28_%=:\n"
    "s_add_u32 s52, s30, 0x9000\n"
    "buffer_load_dwordx4 a[132:135], v45, s[8:11], s52 offen offset:3072 sc1\n"
    "s_add_u32 s52, s30, 0xa000\n"
    "buffer_load_dwordx4 a[136:139], v45, s[8:11], s52 offen offset:0 sc1\n"
    "buffer_load_dwordx4 a[140:143], v45, s[8:11], s52 offen offset:1024 sc1\n"
    "buffer_load_dwordx4 a[144:147], v45, s[8:11], s52 offen offset:2048 sc1\n"
    "buffer_load_dwordx4 a[148:151], v45, s[8:11], s52 offen offset:3072 sc1\n"
    "s_add_u32 s52, s30, 0xb000\n"
    "buffer_load_dwordx4 a[152:155], v45, s[8:11], s52 offen offset:0 sc1\n"
    "buffer_load_dwordx4 a[156:159], v45, s[8:11], s52 offen offset:1024 sc1\n"
    "buffer_load_dwordx4 a[160:163], v45, s[8:11], s52 offen offset:2048 sc1\n"
    "buffer_load_dwordx4 a[164:167], v45, s[8:11], s52 offen offset:3072 sc1\n"
    "s_cmp_eq_u32 s22, 7\n"
    "s_cbranch_scc1 Lskip_31_%=\n"
    "; signal 2: every wave has its write-through stores acknowledged; barrier; one wave stores the flag line\n"
    "s_waitcnt vmcnt(9)\n"
    "s_barrier\n"
    "s_cmp_lg_u32 s20, 0\n"
    "s_cbranch_scc1 Lskip_32_%=\n"
    "s_add_u32 s60, s39, 0x10000\n"
    "s_add_u32 s60, s60, s38\n"
    "s_mov_b64 exec, 0xff\n"
    "buffer_store_dwordx4 v[36:39], v1, s[8:11], s60 offen sc1\n"
    "s_mov_b64 exec, -1\n"
    "Lskip_32_%=:\n"
    "Lskip_31_%=:\n"
    "v_permlane16_swap_b32 v134, v136\n"
    "v_permlane16_swap_b32 v135, v137\n"
    "v_permlane16_swap_b32 v138, v140\n"
    "v_permlane16_swap_b32 v139, v141\n"
    "v_lshlrev_b32 v8, 16, v134\n"
    "v_and_b32 v9, s51, v134\n"
    "v_lshlrev_b32 v10, 16, v135\n"
    "v_and_b32 v11, s51, v135\n"
    "v_lshlrev_b32 v12, 16, v136\n"
    "v_and_b32 v13, s51, v136\n"
    "v_lshlrev_b32 v14, 16, v137\n"
    "v_and_b32 v15, s51, v137\n"
    "v_lshlrev_b32 v16, 16, v138\n"
    "v_and_b32 v17, s51, v138\n"
    "v_lshlrev_b32 v18, 16, v139\n"
    "v_and_b32 v19, s51, v139\n"
    "v_lshlrev_b32 v20, 16, v140\n"
    "v_and_b32 v21, s51, v140\n"
    "v_lshlrev_b32 v22, 16, v141\n"
    "v_and_b32 v23, s51, v141\n"
    "v_mov_b32 v102, s42\n"
    "v_mov_b32 v103, s42\n"
    "v_mov_b32 v104, s42\n"
    "v_mov_b32 v105, s42\n"
    "s_nop 1\n"
    "v_mfma_f32_16x16x32_bf16 v[182:185], a[232:235], v[102:105], 0\n"
    "v_mfma_f32_16x16x32_bf16 v[186:189], a[236:239], v[102:105], 0\n"
    "v_mfma_f32_16x16x32_bf16 v[182:185], a[240:243], v[102:105], v[182:185]\n"
    "v_mfma_f32_16x16x32_bf16 v[186:189], a[244:247], v[102:105], v[186:189]\n"
    "v_mfma_f32_16x16x32_bf16 v[182:185], a[248:251], v[102:105], v[182:185]\n"
    "v_mfma_f32_16x16x32_bf16 v[186:189], a[252:255], v[102:105], v[186:189]\n"
    "v_mfma_f32_16x16x32_bf16 v[182:185], a[32:35], v[102:105], v[182:185]\n"
    "v_mfma_f32_16x16x32_bf16 v[186:189], a[36:39], v[102:105], v[186:189]\n"
    "v_mfma_f32_16x16x32_bf16 v[182:185], a[40:43], v[102:105], v[182:185]\n"
    "v_mfma_f32_16x16x32_bf16 v[186:189], a[44:47], v[102:105], v[186:189]\n"
    "v_mfma_f32_16x16x32_bf16 v[182:185], a[48:51], v[102:105], v[182:185]\n"
    "v_mfma_f32_16x16x32_bf16 v[186:189], a[52:55], v[102:105], v[186:189]\n"
    "v_mfma_f32_16x16x32_bf16 v[182:185], a[56:59], v[102:105], v[182:185]\n"
    "v_mfma_f32_16x16x32_bf16 v[186:189], a[60:63], v[102:105], v[186:189]\n"
    "v_mfma_f32_16x16x32_bf16 v[182:185], a[64:67], v[102:105], v[182:185]\n"
    "v_mfma_f32_16x16x32_bf16 v[186:189], a[68:71], v[102:105], v[186:189]\n"
    "s_waitcnt vmcnt(32)\n"
    "v_mfma_f32_16x16x32_bf16 v[182:185], a[72:75], v[102:105], v[182:185]\n"
    "s_waitcnt vmcnt(31)\n"
    "v_mfma_f32_16x16x32_bf16 v[186:189], a[76:79], v[102:105], v[186:189]\n"
    "s_waitcnt vmcnt(30)\n"
    "v_mfma_f32_16x16x32_bf16 v[182:185], a[80:83], v[102:105], v[182:185]\n"
    "s_waitcnt vmcnt(29)\n"
    "v_mfma_f32_16x16x32_bf16 v[186:189], a[84:87], v[102:105], v[186:189]\n"
    "s_waitcnt vmcnt(28)\n"
    "v_mfma_f32_16x16x32_bf16 v[182:185], a[88:91], v[102:105], v[182:185]\n"
    "s_waitcnt vmcnt(27)\n"
    "v_mfma_f32_16x16x32_bf16 v[186:189], a[92:95], v[102:105], v[186:189]\n"
    "s_waitcnt vmcnt(26)\n"
    "v_mfma_f32_16x16x32_bf16 v[182:185], a[96:99], v[102:105], v[182:185]\n"
    "s_waitcnt vmcnt(25)\n"
    "v_mfma_f32_16x16x32_bf16 v[186:189], a[100:103], v[102:105], v[186:189]\n"
    "s_waitcnt vmcnt(24)\n"
    "v_mfma_f32_16x16x32_bf16 v[182:185], a[104:107], v[102:105], v[182:185]\n"
    "s_waitcnt vmcnt(23)\n"
    "v_mfma_f32_16x16x32_bf16 v[186:189], a[108:111], v[102:105], v[186:189]\n"
    "s_waitcnt vmcnt(22)\n"
    "v_mfma_f32_16x16x32_bf16 v[182:185], a[112:115], v[102:105], v[182:185]\n"
    "s_waitcnt vmcnt(21)\n"
    "v_mfma_f32_16x16x32_bf16 v[186:189], a[116:119], v[102:105], v[186:189]\n"
    "s_add_u32 s52, s30, 0xc000\n"
    "buffer_load_dwordx4 a[168:171], v45, s[8:11], s52 offen offset:0 sc1\n"
    "buffer_load_dwordx4 a[172:175], v45, s[8:11], s52 offen offset:1024 sc1\n"
    "buffer_load_dwordx4 a[176:179], v45, s[8:11], s52 offen offset:2048 sc1\n"
    "buffer_load_dwordx4 a[180:183], v45, s[8:11], s52 offen offset:3072 sc1\n"
    "s_add_u32 s52, s30, 0xd000\n"
    "buffer_load_dwordx4 a[184:187], v45, s[8:11], s52 offen offset:0 sc1\n"
    "buffer_load_dwordx4 a[188:191], v45, s[8:11], s52 offen offset:1024 sc1\n"
    "buffer_load_dwordx4 a[192:195], v45, s[8:11], s52 offen offset:2048 sc1\n"
    "buffer_load_dwordx4 a[196:199], v45, s[8:11], s52 offen offset:3072 sc1\n"
    "s_add_u32 s52, s31, 0x3000\n"
    "buffer_load_dwordx4 a[208:211], v41, s[8:11], s52 offen offset:0 sc1\n"
    "s_add_u32 s71, s38, 0x10000\n"
    "s_waitcnt vmcnt(19)\n"
    "buffer_load_dwordx2 v[46:47], v4, s[8:11], s71 offen sc1\n"
    "buffer_load_dwordx4 a[212:215], v41, s[8:11], s52 offen offset:1024 sc1\n"
    "buffer_load_dwordx4 a[216:219], v41, s[8:11], s52 offen offset:2048 sc1\n"
    "buffer_load_dwordx4 a[220:223], v41, s[8:11], s52 offen offset:3072 sc1\n"
    "s_add_u32 s65, s75, s43\n"
    "s_lshr_b32 s73, s43, 2\n"
    "s_add_u32 s73, s73, s75\n"
    "s_add_u32 s73, s73, 0xe000\n"
    "s_waitcnt vmcnt(3)\n"
    "v_cmp_ne_u64 vcc, s[26:27], v[46:47]\n"
    "s_nop 0\n"
    "s_cbranch_vccnz Lspin_33_%=\n"
    "Lgo_34_%=:\n"
    "s_add_u32 s53, s65, 0x0\n"
    "s_add_u32 m0, s43, 0x0\n"
    "s_nop 0\n"
    "buffer_load_dwordx4 v1, s[8:11], s53 offen offset:0 sc1 lds\n"
    "buffer_load_dwordx4 v1, s[8:11], s53 offen offset:1024 sc1 lds\n"
    "buffer_load_dwordx4 v1, s[8:11], s53 offen offset:2048 sc1 lds\n"
    "buffer_load_dwordx4 v1, s[8:11], s53 offen offset:3072 sc1 lds\n"
    "s_add_u32 s53, s65, 0x1000\n"
    "s_add_u32 m0, s43, 0x1000\n"
    "s_nop 0\n"
    "buffer_load_dwordx4 v1, s[8:11], s53 offen offset:0 sc1 lds\n"
    "buffer_load_dwordx4 v1, s[8:11], s53 offen offset:1024 sc1 lds\n"
    "buffer_load_dwordx4 v1, s[8:11], s53 offen offset:2048 sc1 lds\n"
    "buffer_load_dwordx2 v[88:89], v3, s[8:11], s73 offen offset:0 sc1\n"
    "buffer_load_dwordx2 v[90:91], v3, s[8:11], s73 offen offset:512 sc1\n"
    "buffer_load_dwordx2 v[92:93], v3, s[8:11], s73 offen offset:1024 sc1\n"
    "buffer_load_dwordx2 v[94:95], v3, s[8:11], s73 offen offset:1536 sc1\n"
    "v_mfma_f32_16x16x32_bf16 v[190:193], a[0:3], v[102:105], 0\n"
    "v_mfma_f32_16x16x32_bf16 v[194:197], a[4:7], v[102:105], 0\n"
    "v_mfma_f32_16x16x32_bf16 v[190:193], a[8:11], v[102:105], v[190:193]\n"
    "v_mfma_f32_16x16x32_bf16 v[194:197], a[12:15], v[102:105], v[194:197]\n"
    "v_mfma_f32_16x16x32_bf16 v[190:193], a[16:19], v[102:105], v[190:193]\n"
    "v_mfma_f32_16x16x32_bf16 v[194:197], a[20:23], v[102:105], v[194:197]\n"
    "v_mfma_f32_16x16x32_bf16 v[190:193], a[24:27], v[102:105], v[190:193]\n"
    "v_mfma_f32_16x16x32_bf16 v[194:197], a[28:31], v[102:105], v[194:197]\n"
    "v_mfma_f32_16x16x32_bf16 v[190:193], a[120:123], v[102:105], v[190:193]\n"
    "v_mfma_f32_16x16x32_bf16 v[194:197], a[124:127], v[102:105], v[194:197]\n"
    "v_mfma_f32_16x16x32_bf16 v[190:193], a[128:131], v[102:105], v[190:193]\n"
    "v_mfma_f32_16x16x32_bf16 v[194:197], a[132:135], v[102:105], v[194:197]\n"
    "v_mfma_f32_16x16x32_bf16 v[190:193], a[136:139], v[102:105], v[190:193]\n"
    "v_mfma_f32_16x16x32_bf16 v[194:197], a[140:143], v[102:105], v[194:197]\n"
    "v_mfma_f32_16x16x32_bf16 v[190:193], a[144:147], v[102:105], v[190:193]\n"
    "v_mfma_f32_16x16x32_bf16 v[194:197], a[148:151], v[102:105], v[194:197]\n"
    "v_mfma_f32_16x16x32_bf16 v[190:193], a[152:155], v[102:105], v[190:193]\n"
    "v_mfma_f32_16x16x32_bf16 v[194:197], a[156:159], v[102:105], v[194:197]\n"
    "v_mfma_f32_16x16x32_bf16 v[190:193], a[160:163], v[102:105], v[190:193]\n"
    "v_mfma_f32_16x16x32_bf16 v[194:197], a[164:167], v[102:105], v[194:197]\n"
    "v_mfma_f32_16x16x32_bf16 v[190:193], a[168:171], v[102:105], v[190:193]\n"
    "v_mfma_f32_16x16x32_bf16 v[194:197], a[172:175], v[102:105], v[194:197]\n"
    "v_mfma_f32_16x16x32_bf16 v[190:193], a[176:179], v[102:105], v[190:193]\n"
    "v_mfma_f32_16x16x32_bf16 v[194:197], a[180:183], v[102:105], v[194:197]\n"
    "v_mfma_f32_16x16x32_bf16 v[190:193], a[184:187], v[102:105], v[190:193]\n"
    "v_mfma_f32_16x16x32_bf16 v[194:197], a[188:191], v[102:105], v[194:197]\n"
    "v_mfma_f32_16x16x32_bf16 v[190:193], a[192:195], v[102:105], v[190:193]\n"
    "v_mfma_f32_16x16x32_bf16 v[194:197], a[196:199], v[102:105], v[194:197]\n"
    "; FFN1, column pair 0: h = relu(acc - mean * colsum); the row scale rstd is applied behind the second GEMM\n"
    "buffer_load_dwordx2 v[96:97], v3, s[8:11], s73 offen offset:2048 sc1\n"
    "s_waitcnt vmcnt(10)\n"
    "s_barrier\n"
    "ds_read_b128 v[56:59], v1 offset:0\n"
    "ds_read_b128 v[60:63], v1 offset:14336\n"
    "ds_read_b128 v[64:67], v1 offset:28672\n"
    "ds_read_b128 v[68:71], v1 offset:43008\n"
    "buffer_load_dwordx2 v[98:99], v3, s[8:11], s73 offen offset:2560 sc1\n"
    "buffer_load_dwordx2 v[100:101], v3, s[8:11], s73 offen offset:3072 sc1\n"
    "buffer_load_dwordx4 v1, s[8:11], s53 offen offset:3072 sc1 lds\n"
    "s_waitcnt lgkmcnt(3)\n"
    "v_mfma_f32_16x16x32_bf16 v[118:121], a[232:235], v[56:59], 0\n"
    "ds_read_b128 v[72:75], v1 offset:1024\n"
    "v_mfma_f32_16x16x32_bf16 v[122:125], a[236:239], v[56:59], 0\n"
    "s_waitcnt lgkmcnt(3)\n"
    "v_mfma_f32_16x16x32_bf16 v[126:129], a[232:235], v[60:63], 0\n"
    "ds_read_b128 v[76:79], v1 offset:15360\n"
    "v_mfma_f32_16x16x32_bf16 v[130:133], a[236:239], v[60:63], 0\n"
    "s_add_u32 s53, s65, 0x2000\n"
    "s_add_u32 m0, s43, 0x2000\n"
    "s_nop 0\n"
    "buffer_load_dwordx4 v1, s[8:11], s53 offen offset:0 sc1 lds\n"
    "s_waitcnt lgkmcnt(3)\n"
    "v_mfma_f32_16x16x32_bf16 v[134:137], a[232:235], v[64:67], 0\n"
    "ds_read_b128 v[80:83], v1 offset:29696\n"
    "v_mfma_f32_16x16x32_bf16 v[138:141], a[236:239], v[64:67], 0\n"
    "s_waitcnt lgkmcnt(3)\n"
    "v_mfma_f32_16x16x32_bf16 v[142:145], a[232:235], v[68:71], 0\n"
    "ds_read_b128 v[84:87], v1 offset:44032\n"
    "v_mfma_f32_16x16x32_bf16 v[146:149], a[236:239], v[68:71], 0\n"
    "buffer_load_dwordx4 v1, s[8:11], s53 offen offset:1024 sc1 lds\n"
    "s_waitcnt vmcnt(13)\n"
    "s_barrier\n"
    "s_waitcnt lgkmcnt(3)\n"
    "v_mfma_f32_16x16x32_bf16 v[118:121], a[240:243], v[72:75], v[118:121]\n"
    "ds_read_b128 v[56:59], v1 offset:2048\n"
    "v_mfma_f32_16x16x32_bf16 v[122:125], a[244:247], v[72:75], v[122:125]\n"
    "buffer_load_dwordx4 v1, s[8:11], s53 offen offset:2048 sc1 lds\n"
    "s_waitcnt lgkmcnt(3)\n"
    "v_mfma_f32_16x16x32_bf16 v[126:129], a[240:243], v[76:79], v[126:129]\n"
    "ds_read_b128 v[60:63], v1 offset:16384\n"
    "v_mfma_f32_16x16x32_bf16 v[130:133], a[244:247], v[76:79], v[130:133]\n"
    "buffer_load_dwordx4 v1, s[8:11], s53 offen offset:3072 sc1 lds\n"
    "s_waitcnt lgkmcnt(3)\n"
    "v_mfma_f32_16x16x32_bf16 v[134:137], a[240:243], v[80:83], v[134:137]\n"
    "ds_read_b128 v[64:67], v1 offset:30720\n"
    "v_mfma_f32_16x16x32_bf16 v[138:141], a[244:247], v[80:83], v[138:141]\n"
    "s_add_u32 s53, s65, 0x3000\n"
    "s_add_u32 m0, s43, 0x3000\n"
    "s_nop 0\n"
    "buffer_load_dwordx4 v1, s[8:11], s53 offen offset:0 sc1 lds\n"
    "s_waitcnt lgkmcnt(3)\n"
    "v_mfma_f32_16x16x32_bf16 v[142:145], a[240:243], v[84:87], v[142:145]\n"
    "ds_read_b128 v[68:71], v1 offset:45056\n"
    "v_mfma_f32_16x16x32_bf16 v[146:149], a[244:247], v[84:87], v[146:149]\n"
    "s_waitcnt lgkmcnt(3)\n"
    "v_mfma_f32_16x16x32_bf16 v[118:121], a[248:251], v[56:59], v[118:121]\n"
    "ds_read_b128 v[72:75], v1 offset:3072\n"
    "v_mfma_f32_16x16x32_bf16 v[122:125], a[252:255], v[56:59], v[122:125]\n"
    "buffer_load_dwordx4 v1, s[8:11], s53 offen offset:1024 sc1 lds\n"
    "s_waitcnt lgkmcnt(3)\n"
    "v_mfma_f32_16x16x32_bf16 v[126:129], a[248:251], v[60:63], v[126:129]\n"
    "ds_read_b128 v[76:79], v1 offset:17408\n"
    "v_mfma_f32_16x16x32_bf16 v[130:133], a[252:255], v[60:63], v[130:133]\n"
    "s_waitcnt lgkmcnt(3)\n"
    "v_mfma_f32_16x16x32_bf16 v[134:137], a[248:251], v[64:67], v[134:137]\n"
    "ds_read_b128 v[80:83], v1 offset:31744\n"
    "v_mfma_f32_16x16x32_bf16 v[138:141], a[252:255], v[64:67], v[138:141]\n"
    "s_waitcnt lgkmcnt(3)\n"
    "v_mfma_f32_16x16x32_bf16 v[142:145], a[248:251], v[68:71], v[142:145]\n"
    "ds_read_b128 v[84:87], v1 offset:46080\n"
    "v_mfma_f32_16x16x32_bf16 v[146:149], a[252:255], v[68:71], v[146:149]\n"
    "s_add_u32 s52, s31, 0x4000\n"
    "buffer_load_dwordx4 a[224:227], v41, s[8:11], s52 offen offset:0 sc1\n"
    "s_waitcnt vmcnt(15)\n"
    "s_barrier\n"
    "s_waitcnt lgkmcnt(3)\n"
    "v_mfma_f32_16x16x32_bf16 v[118:121], a[32:35], v[72:75], v[118:121]\n"
    "ds_read_b128 v[56:59], v1 offset:4096\n"
    "v_mfma_f32_16x16x32_bf16 v[122:125], a[36:39], v[72:75], v[122:125]\n"
    "s_waitcnt lgkmcnt(3)\n"
    "v_mfma_f32_16x16x32_bf16 v[126:129], a[32:35], v[76:79], v[126:129]\n"
    "ds_read_b128 v[60:63], v1 offset:18432\n"
    "v_mfma_f32_16x16x32_bf16 v[130:133], a[36:39], v[76:79], v[130:133]\n"
    "buffer_load_dwordx4 a[228:231], v41, s[8:11], s52 offen offset:1024 sc1\n"
    "s_waitcnt lgkmcnt(3)\n"
    "v_mfma_f32_16x16x32_bf16 v[134:137], a[32:35], v[80:83], v[134:137]\n"
    "ds_read_b128 v[64:67], v1 offset:32768\n"
    "v_mfma_f32_16x16x32_bf16 v[138:141], a[36:39], v[80:83], v[138:141]\n"
    "s_waitcnt lgkmcnt(3)\n"
    "v_mfma_f32_16x16x32_bf16 v[142:145], a[32:35], v[84:87], v[142:145]\n"
    "ds_read_b128 v[68:71], v1 offset:47104\n"
    "v_mfma_f32_16x16x32_bf16 v[146:149], a[36:39], v[84:87], v[146:149]\n"
    "buffer_load_dwordx4 a[232:235], v41, s[8:11], s52 offen offset:2048 sc1\n"
    "s_waitcnt lgkmcnt(3)\n"
    "v_mfma_f32_16x16x32_bf16 v[118:121], a[40:43], v[56:59], v[118:121]\n"
    "ds_read_b128 v[72:75], v1 offset:5120\n"
    "v_mfma_f32_16x16x32_bf16 v[122:125], a[44:47], v[56:59], v[122:125]\n"
    "s_waitcnt lgkmcnt(3)\n"
    "v_mfma_f32_16x16x32_bf16 v[126:129], a[40:43], v[60:63], v[126:129]\n"
    "ds_read_b128 v[76:79], v1 offset:19456\n"
    "v_mfma_f32_16x16x32_bf16 v[130:133], a[44:47], v[60:63], v[130:133]\n"
    "buffer_load_dwordx4 a[236:239], v41, s[8:11], s52 offen offset:3072 sc1\n"
    "s_waitcnt lgkmcnt(3)\n"
    "v_mfma_f32_16x16x32_bf16 v[134:137], a[40:43], v[64:67], v[134:137]\n"
    "ds_read_b128 v[80:83], v1 offset:33792\n"
    "v_mfma_f32_16x16x32_bf16 v[138:141], a[44:47], v[64:67], v[138:141]\n"
    "s_waitcnt lgkmcnt(3)\n"
    "v_mfma_f32_16x16x32_bf16 v[142:145], a[40:43], v[68:71], v[142:145]\n"
    "ds_read_b128 v[84:87], v1 offset:48128\n"
    "v_mfma_f32_16x16x32_bf16 v[146:149], a[44:47], v[68:71], v[146:149]\n"
    "s_add_u32 s52, s31, 0x5000\n"
    "buffer_load_dwordx4 a[240:243], v41, s[8:11], s52 offen offset:0 sc1\n"
    "s_waitcnt lgkmcnt(3)\n"
    "v_mfma_f32_16x16x32_bf16 v[118:121], a[48:51], v[72:75], v[118:121]\n"
    "ds_read_b128 v[56:59], v1 offset:6144\n"
    "v_mfma_f32_16x16x32_bf16 v[122:125], a[52:55], v[72:75], v[122:125]\n"
    "s_waitcnt lgkmcnt(3)\n"
    "v_mfma_f32_16x16x32_bf16 v[126:129], a[48:51], v[76:79], v[126:129]\n"
    "ds_read_b128 v[60:63], v1 offset:20480\n"
    "v_mfma_f32_16x16x32_bf16 v[130:133], a[52:55], v[76:79], v[130:133]\n"
    "buffer_load_dwordx4 a[244:247], v41, s[8:11], s52 offen offset:1024 sc1\n"
    "s_waitcnt lgkmcnt(3)\n"
    "v_mfma_f32_16x16x32_bf16 v[134:137], a[48:51], v[80:83], v[134:137]\n"
    "ds_read_b128 v[64:67], v1 offset:34816\n"
    "v_mfma_f32_16x16x32_bf16 v[138:141], a[52:55], v[80:83], v[138:141]\n"
    "s_waitcnt lgkmcnt(3)\n"
    "v_mfma_f32_16x16x32_bf16 v[142:145], a[48:51], v[84:87], v[142:145]\n"
    "ds_read_b128 v[68:71], v1 offset:49152\n"
    "v_mfma_f32_16x16x32_bf16 v[146:149], a[52:55], v[84:87], v[146:149]\n"
    "buffer_load_dwordx4 a[248:251], v41, s[8:11], s52 offen offset:2048 sc1\n"
    "s_waitcnt vmcnt(12)\n"
    "s_barrier\n"
    "s_waitcnt lgkmcnt(3)\n"
    "v_mfma_f32_16x16x32_bf16 v[118:121], a[56:59], v[56:59], v[118:121]\n"
    "ds_read_b128 v[72:75], v1 offset:7168\n"
    "v_mfma_f32_16x16x32_bf16 v[122:125], a[60:63], v[56:59], v[122:125]\n"
    "v_add_f32 v88, v88, v90\n"
    "buffer_load_dwordx4 a[252:255], v41, s[8:11], s52 offen offset:3072 sc1\n"
    "s_waitcnt lgkmcnt(3)\n"
    "v_mfma_f32_16x16x32_bf16 v[126:129], a[56:59], v[60:63], v[126:129]\n"
    "ds_read_b128 v[76:79], v1 offset:21504\n"
    "v_add_f32 v89, v89, v91\n"
    "v_mfma_f32_16x16x32_bf16 v[130:133], a[60:63], v[60:63], v[130:133]\n"
    "v_add_f32 v92, v92, v94\n"
    "s_add_u32 s52, s31, 0x6000\n"
    "buffer_load_dwordx4 a[32:35], v41, s[8:11], s52 offen offset:0 sc1\n"
    "s_waitcnt lgkmcnt(3)\n"
    "v_mfma_f32_16x16x32_bf16 v[134:137], a[56:59], v[64:67], v[134:137]\n"
    "ds_read_b128 v[80:83], v1 offset:35840\n"
    "v_add_f32 v93, v93, v95\n"
    "v_mfma_f32_16x16x32_bf16 v[138:141], a[60:63], v[64:67], v[138:141]\n"
    "v_add_f32 v96, v96, v98\n"
    "s_waitcnt lgkmcnt(3)\n"
    "v_mfma_f32_16x16x32_bf16 v[142:145], a[56:59], v[68:71], v[142:145]\n"
    "ds_read_b128 v[84:87], v1 offset:50176\n"
    "v_add_f32 v97, v97, v99\n"
    "v_mfma_f32_16x16x32_bf16 v[146:149], a[60:63], v[68:71], v[146:149]\n"
    "v_add_f32 v88, v88, v92\n"
    "buffer_load_dwordx4 a[36:39], v41, s[8:11], s52 offen offset:1024 sc1\n"
    "s_waitcnt lgkmcnt(3)\n"
    "v_mfma_f32_16x16x32_bf16 v[118:121], a[64:67], v[72:75], v[118:121]\n"
    "ds_read_b128 v[56:59], v1 offset:8192\n"
    "v_add_f32 v89, v89, v93\n"
    "v_mfma_f32_16x16x32_bf16 v[122:125], a[68:71], v[72:75], v[122:125]\n"
    "v_add_f32 v96, v96, v100\n"
    "s_waitcnt lgkmcnt(3)\n"
    "v_mfma_f32_16x16x32_bf16 v[126:129], a[64:67], v[76:79], v[126:129]\n"
    "ds_read_b128 v[60:63], v1 offset:22528\n"
    "v_add_f32 v97, v97, v101\n"
    "v_mfma_f32_16x16x32_bf16 v[130:133], a[68:71], v[76:79], v[130:133]\n"
    "v_add_f32 v88, v88, v96\n"
    "buffer_load_dwordx4 a[40:43], v41, s[8:11], s52 offen offset:2048 sc1\n"
    "s_waitcnt lgkmcnt(3)\n"
    "v_mfma_f32_16x16x32_bf16 v[134:137], a[64:67], v[80:83], v[134:137]\n"
    "ds_read_b128 v[64:67], v1 offset:36864\n"
    "v_add_f32 v89, v89, v97\n"
    "v_mfma_f32_16x16x32_bf16 v[138:141], a[68:71], v[80:83], v[138:141]\n"
    "ds_write_b64 v43, v[88:89]\n"
    "buffer_load_dwordx4 a[44:47], v41, s[8:11], s52 offen offset:3072 sc1\n"
    "s_waitcnt lgkmcnt(4)\n"
    "v_mfma_f32_16x16x32_bf16 v[142:145], a[64:67], v[84:87], v[142:145]\n"
    "ds_read_b128 v[68:71], v1 offset:51200\n"
    "v_mfma_f32_16x16x32_bf16 v[146:149], a[68:71], v[84:87], v[146:149]\n"
    "s_waitcnt vmcnt(12)\n"
    "s_waitcnt lgkmcnt(1)\n"
    "s_barrier\n"
    "v_mfma_f32_16x16x32_bf16 v[118:121], a[72:75], v[56:59], v[118:121]\n"
    "ds_read_b128 v[72:75], v1 offset:9216\n"
    "v_and_b32 v88, 0xffffffe0, v43\n"
    "v_mfma_f32_16x16x32_bf16 v[122:125], a[76:79], v[56:59], v[122:125]\n"
    "ds_read_b128 v[102:105], v88\n"
    "s_add_u32 s52, s31, 0x7000\n"
    "buffer_load_dwordx4 a[48:51], v41, s[8:11], s52 offen offset:0 sc1\n"
    "v_mfma_f32_16x16x32_bf16 v[126:129], a[72:75], v[60:63], v[126:129]\n"
    "ds_read_b128 v[76:79], v1 offset:23552\n"
    "ds_read_b128 v[106:109], v88 offset:16\n"
    "v_mfma_f32_16x16x32_bf16 v[130:133], a[76:79], v[60:63], v[130:133]\n"
    "buffer_load_dwordx4 a[52:55], v41, s[8:11], s52 offen offset:1024 sc1\n"
    "v_mfma_f32_16x16x32_bf16 v[134:137], a[72:75], v[64:67], v[134:137]\n"
    "ds_read_b128 v[80:83], v1 offset:37888\n"
    "v_mfma_f32_16x16x32_bf16 v[138:141], a[76:79], v[64:67], v[138:141]\n"
    "s_waitcnt lgkmcnt(5)\n"
    "v_mfma_f32_16x16x32_bf16 v[142:145], a[72:75], v[68:71], v[142:145]\n"
    "ds_read_b128 v[84:87], v1 offset:52224\n"
    "v_mfma_f32_16x16x32_bf16 v[146:149], a[76:79], v[68:71], v[146:149]\n"
    "buffer_load_dwordx4 a[56:59], v41, s[8:11], s52 offen offset:2048 sc1\n"
    "s_waitcnt lgkmcnt(5)\n"
    "v_mfma_f32_16x16x32_bf16 v[118:121], a[80:83], v[72:75], v[118:121]\n"
    "ds_read_b128 v[56:59], v1 offset:10240\n"
    "s_waitcnt lgkmcnt(5)\n"
    "v_add_f32 v102, v102, v104\n"
    "v_mfma_f32_16x16x32_bf16 v[122:125], a[84:87], v[72:75], v[122:125]\n"
    "v_add_f32 v103, v103, v105\n"
    "s_waitcnt lgkmcnt(4)\n"
    "v_mfma_f32_16x16x32_bf16 v[126:129], a[80:83], v[76:79], v[126:129]\n"
    "ds_read_b128 v[60:63], v1 offset:24576\n"
    "s_waitcnt lgkmcnt(4)\n"
    "v_add_f32 v106, v106, v108\n"
    "v_mfma_f32_16x16x32_bf16 v[130:133], a[84:87], v[76:79], v[130:133]\n"
    "v_add_f32 v107, v107, v109\n"
    "buffer_load_dwordx4 a[60:63], v41, s[8:11], s52 offen offset:3072 sc1\n"
    "s_waitcnt lgkmcnt(3)\n"
    "v_mfma_f32_16x16x32_bf16 v[134:137], a[80:83], v[80:83], v[134:137]\n"
    "ds_read_b128 v[64:67], v1 offset:38912\n"
    "v_add_f32 v102, v102, v106\n"
    "v_mfma_f32_16x16x32_bf16 v[138:141], a[84:87], v[80:83], v[138:141]\n"
    "v_add_f32 v103, v103, v107\n"
    "s_waitcnt lgkmcnt(3)\n"
    "v_mfma_f32_16x16x32_bf16 v[142:145], a[80:83], v[84:87], v[142:145]\n"
    "ds_read_b128 v[68:71], v1 offset:53248\n"
    "v_mul_f32 v102, 0x3b124925, v102\n"
    "v_mfma_f32_16x16x32_bf16 v[146:149], a[84:87], v[84:87], v[146:149]\n"
    "v_mul_f32 v103, 0x3b124925, v103\n"
    "s_add_u32 s52, s31, 0x8000\n"
    "buffer_load_dwordx4 a[64:67], v41, s[8:11], s52 offen offset:0 sc1\n"
    "s_waitcnt lgkmcnt(3)\n"
    "v_mfma_f32_16x16x32_bf16 v[118:121], a[88:91], v[56:59], v[118:121]\n"
    "ds_read_b128 v[72:75], v1 offset:11264\n"
    "v_fma_f32 v103, -v102, v102, v103\n"
    "v_mfma_f32_16x16x32_bf16 v[122:125], a[92:95], v[56:59], v[122:125]\n"
    "v_max_f32 v103, 0, v103\n"
    "buffer_load_dwordx4 a[68:71], v41, s[8:11], s52 offen offset:1024 sc1\n"
    "s_waitcnt lgkmcnt(3)\n"
    "v_mfma_f32_16x16x32_bf16 v[126:129], a[88:91], v[60:63], v[126:129]\n"
    "ds_read_b128 v[76:79], v1 offset:25600\n"
    "v_add_f32 v103, 0x3727c5ac, v103\n"
    "v_mfma_f32_16x16x32_bf16 v[130:133], a[92:95], v[60:63], v[130:133]\n"
    "v_rsq_f32 v103, v103\n"
    "s_waitcnt lgkmcnt(3)\n"
    "v_mfma_f32_16x16x32_bf16 v[134:137], a[88:91], v[64:67], v[134:137]\n"
    "ds_read_b128 v[80:83], v1 offset:39936\n"
    "v_mfma_f32_16x16x32_bf16 v[138:141], a[92:95], v[64:67], v[138:141]\n"
    "ds_write2st64_b32 v49, v102, v103 offset1:1\n"
    "buffer_load_dwordx4 a[72:75], v41, s[8:11], s52 offen offset:2048 sc1\n"
    "s_waitcnt lgkmcnt(4)\n"
    "v_mfma_f32_16x16x32_bf16 v[142:145], a[88:91], v[68:71], v[142:145]\n"
    "ds_read_b128 v[84:87], v1 offset:54272\n"
    "ds_read2_b32 v[110:111], v50 offset0:0 offset1:64\n"
    "v_mfma_f32_16x16x32_bf16 v[146:149], a[92:95], v[68:71], v[146:149]\n"
    "ds_read2_b32 v[112:113], v50 offset0:16 offset1:80\n"
    "s_waitcnt lgkmcnt(6)\n"
    "v_mfma_f32_16x16x32_bf16 v[118:121], a[96:99], v[72:75], v[118:121]\n"
    "ds_read_b128 v[56:59], v1 offset:12288\n"
    "ds_read2_b32 v[114:115], v50 offset0:32 offset1:96\n"
    "v_mfma_f32_16x16x32_bf16 v[122:125], a[100:103], v[72:75], v[122:125]\n"
    "ds_read2_b32 v[116:117], v50 offset0:48 offset1:112\n"
    "buffer_load_dwordx4 a[76:79], v41, s[8:11], s52 offen offset:3072 sc1\n"
    "s_waitcnt lgkmcnt(8)\n"
    "v_mfma_f32_16x16x32_bf16 v[126:129], a[96:99], v[76:79], v[126:129]\n"
    "ds_read_b128 v[60:63], v1 offset:26624\n"
    "v_mfma_f32_16x16x32_bf16 v[130:133], a[100:103], v[76:79], v[130:133]\n"
    "s_waitcnt lgkmcnt(8)\n"
    "v_mfma_f32_16x16x32_bf16 v[134:137], a[96:99], v[80:83], v[134:137]\n"
    "ds_read_b128 v[64:67], v1 offset:40960\n"
    "v_mfma_f32_16x16x32_bf16 v[138:141], a[100:103], v[80:83], v[138:141]\n"
    "s_add_u32 s52, s31, 0x9000\n"
    "buffer_load_dwordx4 a[80:83], v41, s[8:11], s52 offen offset:0 sc1\n"
    "s_waitcnt lgkmcnt(7)\n"
    "v_mfma_f32_16x16x32_bf16 v[142:145], a[96:99], v[84:87], v[142:145]\n"
    "ds_read_b128 v[68:71], v1 offset:55296\n"
    "v_mfma_f32_16x16x32_bf16 v[146:149], a[100:103], v[84:87], v[146:149]\n"
    "s_waitcnt lgkmcnt(5)\n"
    "v_mfma_f32_16x16x32_bf16 v[118:121], a[104:107], v[56:59], v[118:121]\n"
    "ds_read_b128 v[72:75], v1 offset:13312\n"
    "v_mfma_f32_16x16x32_bf16 v[122:125], a[108:111], v[56:59], v[122:125]\n"
    "s_waitcnt lgkmcnt(3)\n"
    "v_mfma_f32_16x16x32_bf16 v[126:129], a[104:107], v[60:63], v[126:129]\n"
    "ds_read_b128 v[76:79], v1 offset:27648\n"
    "v_mfma_f32_16x16x32_bf16 v[130:133], a[108:111], v[60:63], v[130:133]\n"
    "s_waitcnt lgkmcnt(3)\n"
    "v_mfma_f32_16x16x32_bf16 v[134:137], a[104:107], v[64:67], v[134:137]\n"
    "ds_read_b128 v[80:83], v1 offset:41984\n"
    "v_mfma_f32_16x16x32_bf16 v[138:141], a[108:111], v[64:67], v[138:141]\n"
    "s_waitcnt lgkmcnt(3)\n"
    "v_mfma_f32_16x16x32_bf16 v[142:145], a[104:107], v[68:71], v[142:145]\n"
    "ds_read_b128 v[84:87], v1 offset:56320\n"
    "v_mfma_f32_16x16x32_bf16 v[146:149], a[108:111], v[68:71], v[146:149]\n"
    "s_waitcnt lgkmcnt(3)\n"
    "v_mfma_f32_16x16x32_bf16 v[118:121], a[112:115], v[72:75], v[118:121]\n"
    "ds_read_b128 v[56:59], v1 offset:0\n"
    "v_mfma_f32_16x16x32_bf16 v[122:125], a[116:119], v[72:75], v[122:125]\n"
    "s_waitcnt lgkmcnt(3)\n"
    "v_mfma_f32_16x16x32_bf16 v[126:129], a[112:115], v[76:79], v[126:129]\n"
    "ds_read_b128 v[60:63], v1 offset:14336\n"
    "v_mfma_f32_16x16x32_bf16 v[130:133], a[116:119], v[76:79], v[130:133]\n"
    "s_waitcnt lgkmcnt(3)\n"
    "v_mfma_f32_16x16x32_bf16 v[134:137], a[112:115], v[80:83], v[134:137]\n"
    "ds_read_b128 v[64:67], v1 offset:28672\n"
    "v_mfma_f32_16x16x32_bf16 v[138:141], a[116:119], v[80:83], v[138:141]\n"
    "s_waitcnt lgkmcnt(3)\n"
    "v_mfma_f32_16x16x32_bf16 v[142:145], a[112:115], v[84:87], v[142:145]\n"
    "ds_read_b128 v[68:71], v1 offset:43008\n"
    "v_mfma_f32_16x16x32_bf16 v[146:149], a[116:119], v[84:87], v[146:149]\n"
    "s_lshl_b32 s54, s40, 1\n"
    "s_add_u32 s54, s54, s37\n"
    "v_pk_fma_f32 v[118:119], v[110:111], v[182:183], v[118:119] op_sel_hi:[0,1,1] neg_lo:[1,0,0] neg_hi:[1,0,0]\n"
    "v_pk_fma_f32 v[120:121], v[110:111], v[184:185], v[120:121] op_sel_hi:[0,1,1] neg_lo:[1,0,0] neg_hi:[1,0,0]\n"
    "v_pk_fma_f32 v[122:123], v[110:111], v[186:187], v[122:123] op_sel_hi:[0,1,1] neg_lo:[1,0,0] neg_hi:[1,0,0]\n"
    "v_pk_fma_f32 v[124:125], v[110:111], v[188:189], v[124:125] op_sel_hi:[0,1,1] neg_lo:[1,0,0] neg_hi:[1,0,0]\n"
    "v_cvt_pk_bf16_f32 v198, v118, v119\n"
    "v_cvt_pk_bf16_f32 v199, v120, v121\n"
    "v_cvt_pk_bf16_f32 v200, v122, v123\n"
    "v_cvt_pk_bf16_f32 v201, v124, v125\n"
    "v_pk_max_i16 v198, v198, 0\n"
    "v_pk_max_i16 v199, v199, 0\n"
    "v_pk_max_i16 v200, v200, 0\n"
    "v_pk_max_i16 v201, v201, 0\n"
    "buffer_store_dwordx4 v[198:201], v1, s[8:11], s54 offen sc1\n"
    "s_add_u32 s54, s54, 0xe000\n"
    "v_pk_fma_f32 v[126:127], v[112:113], v[182:183], v[126:127] op_sel_hi:[0,1,1] neg_lo:[1,0,0] neg_hi:[1,0,0]\n"
    "v_pk_fma_f32 v[128:129], v[112:113], v[184:185], v[128:129] op_sel_hi:[0,1,1] neg_lo:[1,0,0] neg_hi:[1,0,0]\n"
    "v_pk_fma_f32 v[130:131], v[112:113], v[186:187], v[130:131] op_sel_hi:[0,1,1] neg_lo:[1,0,0] neg_hi:[1,0,0]\n"
    "v_pk_fma_f32 v[132:133], v[112:113], v[188:189], v[132:133] op_sel_hi:[0,1,1] neg_lo:[1,0,0] neg_hi:[1,0,0]\n"
    "v_cvt_pk_bf16_f32 v202, v126, v127\n"
    "v_cvt_pk_bf16_f32 v203, v128, v129\n"
    "v_cvt_pk_bf16_f32 v204, v130, v131\n"
    "v_cvt_pk_bf16_f32 v205, v132, v133\n"
    "v_pk_max_i16 v202, v202, 0\n"
    "v_pk_max_i16 v203, v203, 0\n"
    "v_pk_max_i16 v204, v204, 0\n"
    "v_pk_max_i16 v205, v205, 0\n"
    "buffer_store_dwordx4 v[202:205], v1, s[8:11], s54 offen sc1\n"
    "s_add_u32 s54, s54, 0xe000\n"
    "v_pk_fma_f32 v[134:135], v[114:115], v[182:183], v[134:135] op_sel_hi:[0,1,1] neg_lo:[1,0,0] neg_hi:[1,0,0]\n"
    "v_pk_fma_f32 v[136:137], v[114:115], v[184:185], v[136:137] op_sel_hi:[0,1,1] neg_lo:[1,0,0] neg_hi:[1,0,0]\n"
    "v_pk_fma_f32 v[138:139], v[114:115], v[186:187], v[138:139] op_sel_hi:[0,1,1] neg_lo:[1,0,0] neg_hi:[1,0,0]\n"
    "v_pk_fma_f32 v[140:141], v[114:115], v[188:189], v[140:141] op_sel_hi:[0,1,1] neg_lo:[1,0,0] neg_hi:[1,0,0]\n"
    "v_cvt_pk_bf16_f32 v206, v134, v135\n"
    "v_cvt_pk_bf16_f32 v207, v136, v137\n"
    "v_cvt_pk_bf16_f32 v208, v138, v139\n"
    "v_cvt_pk_bf16_f32 v209, v140, v141\n"
    "v_pk_max_i16 v206, v206, 0\n"
    "v_pk_max_i16 v207, v207, 0\n"
    "v_pk_max_i16 v208, v208, 0\n"
    "v_pk_max_i16 v209, v209, 0\n"
    "buffer_store_dwordx4 v[206:209], v1, s[8:11], s54 offen sc1\n"
    "s_add_u32 s54, s54, 0xe000\n"
    "v_pk_fma_f32 v[142:143], v[116:117], v[182:183], v[142:143] op_sel_hi:[0,1,1] neg_lo:[1,0,0] neg_hi:[1,0,0]\n"
    "v_pk_fma_f32 v[144:145], v[116:117], v[184:185], v[144:145] op_sel_hi:[0,1,1] neg_lo:[1,0,0] neg_hi:[1,0,0]\n"
    "v_pk_fma_f32 v[146:147], v[116:117], v[186:187], v[146:147] op_sel_hi:[0,1,1] neg_lo:[1,0,0] neg_hi:[1,0,0]\n"
    "v_pk_fma_f32 v[148:149], v[116:117], v[188:189], v[148:149] op_sel_hi:[0,1,1] neg_lo:[1,0,0] neg_hi:[1,0,0]\n"
    "v_cvt_pk_bf16_f32 v210, v142, v143\n"
    "v_cvt_pk_bf16_f32 v211, v144, v145\n"
    "v_cvt_pk_bf16_f32 v212, v146, v147\n"
    "v_cvt_pk_bf16_f32 v213, v148, v149\n"
    "v_pk_max_i16 v210, v210, 0\n"
    "v_pk_max_i16 v211, v211, 0\n"
    "v_pk_max_i16 v212, v212, 0\n"
    "v_pk_max_i16 v213, v213, 0\n"
    "buffer_store_dwordx4 v[210:213], v1, s[8:11], s54 offen sc1\n"
    "s_mul_i32 s65, s20, 0xe000\n"
    "s_add_u32 s65, s65, s37\n"
    "; FFN1, column pair 1: h = relu(acc - mean * colsum); the row scale rstd is applied behind the second GEMM\n"
    "s_waitcnt lgkmcnt(3)\n"
    "v_mfma_f32_16x16x32_bf16 v[150:153], a[0:3], v[56:59], 0\n"
    "ds_read_b128 v[72:75], v1 offset:1024\n"
    "v_mfma_f32_16x16x32_bf16 v[154:157], a[4:7], v[56:59], 0\n"
    "buffer_load_dwordx4 a[84:87], v41, s[8:11], s52 offen offset:1024 sc1\n"
    "s_waitcnt lgkmcnt(3)\n"
    "v_mfma_f32_16x16x32_bf16 v[158:161], a[0:3], v[60:63], 0\n"
    "ds_read_b128 v[76:79], v1 offset:15360\n"
    "v_mfma_f32_16x16x32_bf16 v[162:165], a[4:7], v[60:63], 0\n"
    "buffer_load_dwordx4 a[88:91], v41, s[8:11], s52 offen offset:2048 sc1\n"
    "s_waitcnt lgkmcnt(3)\n"
    "v_mfma_f32_16x16x32_bf16 v[166:169], a[0:3], v[64:67], 0\n"
    "ds_read_b128 v[80:83], v1 offset:29696\n"
    "v_mfma_f32_16x16x32_bf16 v[170:173], a[4:7], v[64:67], 0\n"
    "buffer_load_dwordx4 a[92:95], v41, s[8:11], s52 offen offset:3072 sc1\n"
    "s_waitcnt lgkmcnt(3)\n"
    "v_mfma_f32_16x16x32_bf16 v[174:177], a[0:3], v[68:71], 0\n"
    "ds_read_b128 v[84:87], v1 offset:44032\n"
    "v_mfma_f32_16x16x32_bf16 v[178:181], a[4:7], v[68:71], 0\n"
    "s_add_u32 s52, s31, 0xa000\n"
    "buffer_load_dwordx4 a[96:99], v41, s[8:11], s52 offen offset:0 sc1\n"
    "s_waitcnt lgkmcnt(3)\n"
    "v_mfma_f32_16x16x32_bf16 v[150:153], a[8:11], v[72:75], v[150:153]\n"
    "ds_read_b128 v[56:59], v1 offset:2048\n"
    "v_mfma_f32_16x16x32_bf16 v[154:157], a[12:15], v[72:75], v[154:157]\n"
    "buffer_load_dwordx4 a[100:103], v41, s[8:11], s52 offen offset:1024 sc1\n"
    "s_waitcnt lgkmcnt(3)\n"
    "v_mfma_f32_16x16x32_bf16 v[158:161], a[8:11], v[76:79], v[158:161]\n"
    "ds_read_b128 v[60:63], v1 offset:16384\n"
    "v_mfma_f32_16x16x32_bf16 v[162:165], a[12:15], v[76:79], v[162:165]\n"
    "buffer_load_dwordx4 a[104:107], v41, s[8:11], s52 offen offset:2048 sc1\n"
    "s_waitcnt lgkmcnt(3)\n"
    "v_mfma_f32_16x16x32_bf16 v[166:169], a[8:11], v[80:83], v[166:169]\n"
    "ds_read_b128 v[64:67], v1 offset:30720\n"
    "v_mfma_f32_16x16x32_bf16 v[170:173], a[12:15], v[80:83], v[170:173]\n"
    "buffer_load_dwordx4 a[108:111], v41, s[8:11], s52 offen offset:3072 sc1\n"
    "s_waitcnt lgkmcnt(3)\n"
    "v_mfma_f32_16x16x32_bf16 v[174:177], a[8:11], v[84:87], v[174:177]\n"
    "ds_read_b128 v[68:71], v1 offset:45056\n"
    "v_mfma_f32_16x16x32_bf16 v[178:181], a[12:15], v[84:87], v[178:181]\n"
    "s_add_u32 s52, s31, 0xb000\n"
    "buffer_load_dwordx4 a[112:115], v41, s[8:11], s52 offen offset:0 sc1\n"
    "s_waitcnt lgkmcnt(3)\n"
    "v_mfma_f32_16x16x32_bf16 v[150:153], a[16:19], v[56:59], v[150:153]\n"
    "ds_read_b128 v[72:75], v1 offset:3072\n"
    "v_mfma_f32_16x16x32_bf16 v[154:157], a[20:23], v[56:59], v[154:157]\n"
    "buffer_load_dwordx4 a[116:119], v41, s[8:11], s52 offen offset:1024 sc1\n"
    "s_waitcnt lgkmcnt(3)\n"
    "v_mfma_f32_16x16x32_bf16 v[158:161], a[16:19], v[60:63], v[158:161]\n"
    "ds_read_b128 v[76:79], v1 offset:17408\n"
    "v_mfma_f32_16x16x32_bf16 v[162:165], a[20:23], v[60:63], v[162:165]\n"
    "s_waitcnt lgkmcnt(3)\n"
    "v_mfma_f32_16x16x32_bf16 v[166:169], a[16:19], v[64:67], v[166:169]\n"
    "ds_read_b128 v[80:83], v1 offset:31744\n"
    "v_mfma_f32_16x16x32_bf16 v[170:173], a[20:23], v[64:67], v[170:173]\n"
    "s_waitcnt lgkmcnt(3)\n"
    "v_mfma_f32_16x16x32_bf16 v[174:177], a[16:19], v[68:71], v[174:177]\n"
    "ds_read_b128 v[84:87], v1 offset:46080\n"
    "v_mfma_f32_16x16x32_bf16 v[178:181], a[20:23], v[68:71], v[178:181]\n"
    "s_waitcnt lgkmcnt(3)\n"
    "v_mfma_f32_16x16x32_bf16 v[150:153], a[24:27], v[72:75], v[150:153]\n"
    "ds_read_b128 v[56:59], v1 offset:4096\n"
    "; signal 3: every wave has its write-through stores acknowledged; barrier; one wave stores the flag line\n"
    "s_waitcnt vmcnt(9)\n"
    "s_barrier\n"
    "s_cmp_lg_u32 s20, 0\n"
    "s_cbranch_scc1 Lskip_35_%=\n"
    "s_add_u32 s60, s39, 0x18000\n"
    "s_add_u32 s60, s60, s38\n"
    "s_mov_b64 exec, 0xff\n"
    "buffer_store_dwordx4 v[36:39], v1, s[8:11], s60 offen sc1\n"
    "s_mov_b64 exec, -1\n"
    "Lskip_35_%=:\n"
    "v_mfma_f32_16x16x32_bf16 v[154:157], a[28:31], v[72:75], v[154:157]\n"
    "s_waitcnt lgkmcnt(3)\n"
    "v_mfma_f32_16x16x32_bf16 v[158:161], a[24:27], v[76:79], v[158:161]\n"
    "ds_read_b128 v[60:63], v1 offset:18432\n"
    "v_mfma_f32_16x16x32_bf16 v[162:165], a[28:31], v[76:79], v[162:165]\n"
    "s_waitcnt lgkmcnt(3)\n"
    "v_mfma_f32_16x16x32_bf16 v[166:169], a[24:27], v[80:83], v[166:169]\n"
    "ds_read_b128 v[64:67], v1 offset:32768\n"
    "v_mfma_f32_16x16x32_bf16 v[170:173], a[28:31], v[80:83], v[170:173]\n"
    "s_waitcnt lgkmcnt(3)\n"
    "v_mfma_f32_16x16x32_bf16 v[174:177], a[24:27], v[84:87], v[174:177]\n"
    "ds_read_b128 v[68:71], v1 offset:47104\n"
    "v_mfma_f32_16x16x32_bf16 v[178:181], a[28:31], v[84:87], v[178:181]\n"
    "s_waitcnt lgkmcnt(3)\n"
    "v_mfma_f32_16x16x32_bf16 v[150:153], a[120:123], v[56:59], v[150:153]\n"
    "ds_read_b128 v[72:75], v1 offset:5120\n"
    "v_mfma_f32_16x16x32_bf16 v[154:157], a[124:127], v[56:59], v[154:157]\n"
    "s_waitcnt lgkmcnt(3)\n"
    "v_mfma_f32_16x16x32_bf16 v[158:161], a[120:123], v[60:63], v[158:161]\n"
    "ds_read_b128 v[76:79], v1 offset:19456\n"
    "v_mfma_f32_16x16x32_bf16 v[162:165], a[124:127], v[60:63], v[162:165]\n"
    "s_waitcnt lgkmcnt(3)\n"
    "v_mfma_f32_16x16x32_bf16 v[166:169], a[120:123], v[64:67], v[166:169]\n"
    "ds_read_b128 v[80:83], v1 offset:33792\n"
    "v_mfma_f32_16x16x32_bf16 v[170:173], a[124:127], v[64:67], v[170:173]\n"
    "s_waitcnt lgkmcnt(3)\n"
    "v_mfma_f32_16x16x32_bf16 v[174:177], a[120:123], v[68:71], v[174:177]\n"
    "ds_read_b128 v[84:87], v1 offset:48128\n"
    "v_mfma_f32_16x16x32_bf16 v[178:181], a[124:127], v[68:71], v[178:181]\n"
    "buffer_load_dwordx4 a[120:123], v41, s[8:11], s52 offen offset:2048 sc1\n"
    "s_waitcnt lgkmcnt(3)\n"
    "v_mfma_f32_16x16x32_bf16 v[150:153], a[128:131], v[72:75], v[150:153]\n"
    "ds_read_b128 v[56:59], v1 offset:6144\n"
    "v_mfma_f32_16x16x32_bf16 v[154:157], a[132:135], v[72:75], v[154:157]\n"
    "buffer_load_dwordx4 a[124:127], v41, s[8:11], s52 offen offset:3072 sc1\n"
    "s_waitcnt lgkmcnt(3)\n"
    "v_mfma_f32_16x16x32_bf16 v[158:161], a[128:131], v[76:79], v[158:161]\n"
    "ds_read_b128 v[60:63], v1 offset:20480\n"
    "v_mfma_f32_16x16x32_bf16 v[162:165], a[132:135], v[76:79], v[162:165]\n"
    "s_waitcnt lgkmcnt(3)\n"
    "v_mfma_f32_16x16x32_bf16 v[166:169], a[128:131], v[80:83], v[166:169]\n"
    "ds_read_b128 v[64:67], v1 offset:34816\n"
    "v_mfma_f32_16x16x32_bf16 v[170:173], a[132:135], v[80:83], v[170:173]\n"
    "s_waitcnt lgkmcnt(3)\n"
    "v_mfma_f32_16x16x32_bf16 v[174:177], a[128:131], v[84:87], v[174:177]\n"
    "ds_read_b128 v[68:71], v1 offset:49152\n"
    "v_mfma_f32_16x16x32_bf16 v[178:181], a[132:135], v[84:87], v[178:181]\n"
    "s_add_u32 s52, s31, 0xc000\n"
    "buffer_load_dwordx4 a[128:131], v41, s[8:11], s52 offen offset:0 sc1\n"
    "s_waitcnt lgkmcnt(3)\n"
    "v_mfma_f32_16x16x32_bf16 v[150:153], a[136:139], v[56:59], v[150:153]\n"
    "ds_read_b128 v[72:75], v1 offset:7168\n"
    "v_mfma_f32_16x16x32_bf16 v[154:157], a[140:143], v[56:59], v[154:157]\n"
    "buffer_load_dwordx4 a[132:135], v41, s[8:11], s52 offen offset:1024 sc1\n"
    "s_waitcnt lgkmcnt(3)\n"
    "v_mfma_f32_16x16x32_bf16 v[158:161], a[136:139], v[60:63], v[158:161]\n"
    "ds_read_b128 v[76:79], v1 offset:21504\n"
    "v_mfma_f32_16x16x32_bf16 v[162:165], a[140:143], v[60:63], v[162:165]\n"
    "s_waitcnt lgkmcnt(3)\n"
    "v_mfma_f32_16x16x32_bf16 v[166:169], a[136:139], v[64:67], v[166:169]\n"
    "ds_read_b128 v[80:83], v1 offset:35840\n"
    "v_mfma_f32_16x16x32_bf16 v[170:173], a[140:143], v[64:67], v[170:173]\n"
    "s_waitcnt lgkmcnt(3)\n"
    "v_mfma_f32_16x16x32_bf16 v[174:177], a[136:139], v[68:71], v[174:177]\n"
    "ds_read_b128 v[84:87], v1 offset:50176\n"
    "v_mfma_f32_16x16x32_bf16 v[178:181], a[140:143], v[68:71], v[178:181]\n"
    "buffer_load_dwordx4 a[136:139], v41, s[8:11], s52 offen offset:2048 sc1\n"
    "s_waitcnt lgkmcnt(3)\n"
    "v_mfma_f32_16x16x32_bf16 v[150:153], a[144:147], v[72:75], v[150:153]\n"
    "ds_read_b128 v[56:59], v1 offset:8192\n"
    "v_mfma_f32_16x16x32_bf16 v[154:157], a[148:151], v[72:75], v[154:157]\n"
    "buffer_load_dwordx4 a[140:143], v41, s[8:11], s52 offen offset:3072 sc1\n"
    "s_waitcnt lgkmcnt(3)\n"
    "v_mfma_f32_16x16x32_bf16 v[158:161], a[144:147], v[76:79], v[158:161]\n"
    "ds_read_b128 v[60:63], v1 offset:22528\n"
    "v_mfma_f32_16x16x32_bf16 v[162:165], a[148:151], v[76:79], v[162:165]\n"
    "s_waitcnt lgkmcnt(3)\n"
    "v_mfma_f32_16x16x32_bf16 v[166:169], a[144:147], v[80:83], v[166:169]\n"
    "ds_read_b128 v[64:67], v1 offset:36864\n"
    "v_mfma_f32_16x16x32_bf16 v[170:173], a[148:151], v[80:83], v[170:173]\n"
    "s_waitcnt lgkmcnt(3)\n"
    "v_mfma_f32_16x16x32_bf16 v[174:177], a[144:147], v[84:87], v[174:177]\n"
    "ds_read_b128 v[68:71], v1 offset:51200\n"
    "v_mfma_f32_16x16x32_bf16 v[178:181], a[148:151], v[84:87], v[178:181]\n"
    "s_add_u32 s52, s31, 0xd000\n"
    "buffer_load_dwordx4 a[144:147], v41, s[8:11], s52 offen offset:0 sc1\n"
    "s_waitcnt lgkmcnt(3)\n"
    "v_mfma_f32_16x16x32_bf16 v[150:153], a[152:155], v[56:59], v[150:153]\n"
    "ds_read_b128 v[72:75], v1 offset:9216\n"
    "v_mfma_f32_16x16x32_bf16 v[154:157], a[156:159], v[56:59], v[154:157]\n"
    "buffer_load_dwordx4 a[148:151], v41, s[8:11], s52 offen offset:1024 sc1\n"
    "s_waitcnt lgkmcnt(3)\n"
    "v_mfma_f32_16x16x32_bf16 v[158:161], a[152:155], v[60:63], v[158:161]\n"
    "ds_read_b128 v[76:79], v1 offset:23552\n"
    "v_mfma_f32_16x16x32_bf16 v[162:165], a[156:159], v[60:63], v[162:165]\n"
    "s_add_u32 s71, s38, 0x18000\n"
    "buffer_load_dwordx2 v[46:47], v5, s[8:11], s71 offen sc1\n"
    "s_waitcnt lgkmcnt(3)\n"
    "v_mfma_f32_16x16x32_bf16 v[166:169], a[152:155], v[64:67], v[166:169]\n"
    "ds_read_b128 v[80:83], v1 offset:37888\n"
    "v_mfma_f32_16x16x32_bf16 v[170:173], a[156:159], v[64:67], v[170:173]\n"
    "s_waitcnt lgkmcnt(3)\n"
    "v_mfma_f32_16x16x32_bf16 v[174:177], a[152:155], v[68:71], v[174:177]\n"
    "ds_read_b128 v[84:87], v1 offset:52224\n"
    "v_mfma_f32_16x16x32_bf16 v[178:181], a[156:159], v[68:71], v[178:181]\n"
    "buffer_load_dwordx4 a[152:155], v41, s[8:11], s52 offen offset:2048 sc1\n"
    "s_waitcnt lgkmcnt(3)\n"
    "v_mfma_f32_16x16x32_bf16 v[150:153], a[160:163], v[72:75], v[150:153]\n"
    "ds_read_b128 v[56:59], v1 offset:10240\n"
    "v_mfma_f32_16x16x32_bf16 v[154:157], a[164:167], v[72:75], v[154:157]\n"
    "buffer_load_dwordx4 a[156:159], v41, s[8:11], s52 offen offset:3072 sc1\n"
    "s_waitcnt lgkmcnt(3)\n"
    "v_mfma_f32_16x16x32_bf16 v[158:161], a[160:163], v[76:79], v[158:161]\n"
    "ds_read_b128 v[60:63], v1 offset:24576\n"
    "v_mfma_f32_16x16x32_bf16 v[162:165], a[164:167], v[76:79], v[162:165]\n"
    "s_waitcnt lgkmcnt(3)\n"
    "v_mfma_f32_16x16x32_bf16 v[166:169], a[160:163], v[80:83], v[166:169]\n"
    "ds_read_b128 v[64:67], v1 offset:38912\n"
    "v_mfma_f32_16x16x32_bf16 v[170:173], a[164:167], v[80:83], v[170:173]\n"
    "s_waitcnt lgkmcnt(3)\n"
    "v_mfma_f32_16x16x32_bf16 v[174:177], a[160:163], v[84:87], v[174:177]\n"
    "ds_read_b128 v[68:71], v1 offset:53248\n"
    "v_mfma_f32_16x16x32_bf16 v[178:181], a[164:167], v[84:87], v[178:181]\n"
    "s_waitcnt lgkmcnt(3)\n"
    "v_mfma_f32_16x16x32_bf16 v[150:153], a[168:171], v[56:59], v[150:153]\n"
    "ds_read_b128 v[72:75], v1 offset:11264\n"
    "v_mfma_f32_16x16x32_bf16 v[154:157], a[172:175], v[56:59], v[154:157]\n"
    "s_waitcnt lgkmcnt(3)\n"
    "v_mfma_f32_16x16x32_bf16 v[158:161], a[168:171], v[60:63], v[158:161]\n"
    "ds_read_b128 v[76:79], v1 offset:25600\n"
    "v_mfma_f32_16x16x32_bf16 v[162:165], a[172:175], v[60:63], v[162:165]\n"
    "s_waitcnt lgkmcnt(3)\n"
    "v_mfma_f32_16x16x32_bf16 v[166:169], a[168:171], v[64:67], v[166:169]\n"
    "ds_read_b128 v[80:83], v1 offset:39936\n"
    "v_mfma_f32_16x16x32_bf16 v[170:173], a[172:175], v[64:67], v[170:173]\n"
    "s_waitcnt lgkmcnt(3)\n"
    "v_mfma_f32_16x16x32_bf16 v[174:177], a[168:171], v[68:71], v[174:177]\n"
    "ds_read_b128 v[84:87], v1 offset:54272\n"
    "v_mfma_f32_16x16x32_bf16 v[178:181], a[172:175], v[68:71], v[178:181]\n"
    "s_waitcnt lgkmcnt(3)\n"
    "v_mfma_f32_16x16x32_bf16 v[150:153], a[176:179], v[72:75], v[150:153]\n"
    "ds_read_b128 v[56:59], v1 offset:12288\n"
    "v_mfma_f32_16x16x32_bf16 v[154:157], a[180:183], v[72:75], v[154:157]\n"
    "s_waitcnt lgkmcnt(3)\n"
    "v_mfma_f32_16x16x32_bf16 v[158:161], a[176:179], v[76:79], v[158:161]\n"
    "ds_read_b128 v[60:63], v1 offset:26624\n"
    "v_mfma_f32_16x16x32_bf16 v[162:165], a[180:183], v[76:79], v[162:165]\n"
    "s_waitcnt lgkmcnt(3)\n"
    "v_mfma_f32_16x16x32_bf16 v[166:169], a[176:179], v[80:83], v[166:169]\n"
    "ds_read_b128 v[64:67], v1 offset:40960\n"
    "v_mfma_f32_16x16x32_bf16 v[170:173], a[180:183], v[80:83], v[170:173]\n"
    "s_waitcnt lgkmcnt(3)\n"
    "v_mfma_f32_16x16x32_bf16 v[174:177], a[176:179], v[84:87], v[174:177]\n"
    "ds_read_b128 v[68:71], v1 offset:55296\n"
    "v_mfma_f32_16x16x32_bf16 v[178:181], a[180:183], v[84:87], v[178:181]\n"
    "s_waitcnt lgkmcnt(3)\n"
    "v_mfma_f32_16x16x32_bf16 v[150:153], a[184:187], v[56:59], v[150:153]\n"
    "ds_read_b128 v[72:75], v1 offset:13312\n"
    "v_mfma_f32_16x16x32_bf16 v[154:157], a[188:191], v[56:59], v[154:157]\n"
    "s_waitcnt lgkmcnt(3)\n"
    "v_mfma_f32_16x16x32_bf16 v[158:161], a[184:187], v[60:63], v[158:161]\n"
    "ds_read_b128 v[76:79], v1 offset:27648\n"
    "v_mfma_f32_16x16x32_bf16 v[162:165], a[188:191], v[60:63], v[162:165]\n"
    "s_waitcnt lgkmcnt(3)\n"
    "v_mfma_f32_16x16x32_bf16 v[166:169], a[184:187], v[64:67], v[166:169]\n"
    "ds_read_b128 v[80:83], v1 offset:41984\n"
    "v_mfma_f32_16x16x32_bf16 v[170:173], a[188:191], v[64:67], v[170:173]\n"
    "s_waitcnt lgkmcnt(3)\n"
    "v_mfma_f32_16x16x32_bf16 v[174:177], a[184:187], v[68:71], v[174:177]\n"
    "ds_read_b128 v[84:87], v1 offset:56320\n"
    "v_mfma_f32_16x16x32_bf16 v[178:181], a[188:191], v[68:71], v[178:181]\n"
    "s_waitcnt lgkmcnt(3)\n"
    "v_mfma_f32_16x16x32_bf16 v[150:153], a[192:195], v[72:75], v[150:153]\n"
    "v_mfma_f32_16x16x32_bf16 v[154:157], a[196:199], v[72:75], v[154:157]\n"
    "s_waitcnt lgkmcnt(2)\n"
    "v_mfma_f32_16x16x32_bf16 v[158:161], a[192:195], v[76:79], v[158:161]\n"
    "v_mfma_f32_16x16x32_bf16 v[162:165], a[196:199], v[76:79], v[162:165]\n"
    "s_waitcnt lgkmcnt(1)\n"
    "v_mfma_f32_16x16x32_bf16 v[166:169], a[192:195], v[80:83], v[166:169]\n"
    "v_mfma_f32_16x16x32_bf16 v[170:173], a[196:199], v[80:83], v[170:173]\n"
    "s_waitcnt lgkmcnt(0)\n"
    "v_mfma_f32_16x16x32_bf16 v[174:177], a[192:195], v[84:87], v[174:177]\n"
    "v_mfma_f32_16x16x32_bf16 v[178:181], a[196:199], v[84:87], v[178:181]\n"
    "s_add_u32 s54, s78, s37\n"
    "v_pk_fma_f32 v[150:151], v[110:111], v[190:191], v[150:151] op_sel_hi:[0,1,1] neg_lo:[1,0,0] neg_hi:[1,0,0]\n"
    "v_pk_fma_f32 v[152:153], v[110:111], v[192:193], v[152:153] op_sel_hi:[0,1,1] neg_lo:[1,0,0] neg_hi:[1,0,0]\n"
    "v_pk_fma_f32 v[154:155], v[110:111], v[194:195], v[154:155] op_sel_hi:[0,1,1] neg_lo:[1,0,0] neg_hi:[1,0,0]\n"
    "v_pk_fma_f32 v[156:157], v[110:111], v[196:197], v[156:157] op_sel_hi:[0,1,1] neg_lo:[1,0,0] neg_hi:[1,0,0]\n"
    "v_cvt_pk_bf16_f32 v198, v150, v151\n"
    "v_cvt_pk_bf16_f32 v199, v152, v153\n"
    "v_cvt_pk_bf16_f32 v200, v154, v155\n"
    "v_cvt_pk_bf16_f32 v201, v156, v157\n"
    "v_pk_max_i16 v198, v198, 0\n"
    "v_pk_max_i16 v199, v199, 0\n"
    "v_pk_max_i16 v200, v200, 0\n"
    "v_pk_max_i16 v201, v201, 0\n"
    "buffer_store_dwordx4 v[198:201], v45, s[8:11], s54 offen sc1\n"
    "s_add_u32 s54, s54, 0xe000\n"
    "v_pk_fma_f32 v[158:159], v[112:113], v[190:191], v[158:159] op_sel_hi:[0,1,1] neg_lo:[1,0,0] neg_hi:[1,0,0]\n"
    "v_pk_fma_f32 v[160:161], v[112:113], v[192:193], v[160:161] op_sel_hi:[0,1,1] neg_lo:[1,0,0] neg_hi:[1,0,0]\n"
    "v_pk_fma_f32 v[162:163], v[112:113], v[194:195], v[162:163] op_sel_hi:[0,1,1] neg_lo:[1,0,0] neg_hi:[1,0,0]\n"
    "v_pk_fma_f32 v[164:165], v[112:113], v[196:197], v[164:165] op_sel_hi:[0,1,1] neg_lo:[1,0,0] neg_hi:[1,0,0]\n"
    "v_cvt_pk_bf16_f32 v202, v158, v159\n"
    "v_cvt_pk_bf16_f32 v203, v160, v161\n"
    "v_cvt_pk_bf16_f32 v204, v162, v163\n"
    "v_cvt_pk_bf16_f32 v205, v164, v165\n"
    "v_pk_max_i16 v202, v202, 0\n"
    "v_pk_max_i16 v203, v203, 0\n"
    "v_pk_max_i16 v204, v204, 0\n"
    "v_pk_max_i16 v205, v205, 0\n"
    "buffer_store_dwordx4 v[202:205], v45, s[8:11], s54 offen sc1\n"
    "s_add_u32 s54, s54, 0xe000\n"
    "v_pk_fma_f32 v[166:167], v[114:115], v[190:191], v[166:167] op_sel_hi:[0,1,1] neg_lo:[1,0,0] neg_hi:[1,0,0]\n"
    "v_pk_fma_f32 v[168:169], v[114:115], v[192:193], v[168:169] op_sel_hi:[0,1,1] neg_lo:[1,0,0] neg_hi:[1,0,0]\n"
    "v_pk_fma_f32 v[170:171], v[114:115], v[194:195], v[170:171] op_sel_hi:[0,1,1] neg_lo:[1,0,0] neg_hi:[1,0,0]\n"
    "v_pk_fma_f32 v[172:173], v[114:115], v[196:197], v[172:173] op_sel_hi:[0,1,1] neg_lo:[1,0,0] neg_hi:[1,0,0]\n"
    "v_cvt_pk_bf16_f32 v206, v166, v167\n"
    "v_cvt_pk_bf16_f32 v207, v168, v169\n"
    "v_cvt_pk_bf16_f32 v208, v170, v171\n"
    "v_cvt_pk_bf16_f32 v209, v172, v173\n"
    "v_pk_max_i16 v206, v206, 0\n"
    "v_pk_max_i16 v207, v207, 0\n"
    "v_pk_max_i16 v208, v208, 0\n"
    "v_pk_max_i16 v209, v209, 0\n"
    "buffer_store_dwordx4 v[206:209], v45, s[8:11], s54 offen sc1\n"
    "s_add_u32 s54, s54, 0xe000\n"
    "v_pk_fma_f32 v[174:175], v[116:117], v[190:191], v[174:175] op_sel_hi:[0,1,1] neg_lo:[1,0,0] neg_hi:[1,0,0]\n"
    "v_pk_fma_f32 v[176:177], v[116:117], v[192:193], v[176:177] op_sel_hi:[0,1,1] neg_lo:[1,0,0] neg_hi:[1,0,0]\n"
    "v_pk_fma_f32 v[178:179], v[116:117], v[194:195], v[178:179] op_sel_hi:[0,1,1] neg_lo:[1,0,0] neg_hi:[1,0,0]\n"
    "v_pk_fma_f32 v[180:181], v[116:117], v[196:197], v[180:181] op_sel_hi:[0,1,1] neg_lo:[1,0,0] neg_hi:[1,0,0]\n"
    "v_cvt_pk_bf16_f32 v210, v174, v175\n"
    "v_cvt_pk_bf16_f32 v211, v176, v177\n"
    "v_cvt_pk_bf16_f32 v212, v178, v179\n"
    "v_cvt_pk_bf16_f32 v213, v180, v181\n"
    "v_pk_max_i16 v210, v210, 0\n"
    "v_pk_max_i16 v211, v211, 0\n"
    "v_pk_max_i16 v212, v212, 0\n"
    "v_pk_max_i16 v213, v213, 0\n"
    "buffer_store_dwordx4 v[210:213], v45, s[8:11], s54 offen sc1\n"
    "s_cmp_eq_u32 s22, 7\n"
    "s_cbranch_scc1 Lnog_36_%=\n"
    "s_waitcnt vmcnt(6)\n"
    "v_cmp_ne_u64 vcc, s[26:27], v[46:47]\n"
    "s_nop 0\n"
    "s_cbranch_vccnz Lspin_37_%=\n"
    "Lgo_38_%=:\n"
    "; FFN2\n"
    "s_add_u32 s53, s65, 0x0\n"
    "s_add_u32 m0, s25, 0x0\n"
    "s_nop 0\n"
    "buffer_load_dwordx4 v1, s[8:11], s53 offen offset:0 sc1 lds\n"
    "buffer_load_dwordx4 v1, s[8:11], s53 offen offset:1024 sc1 lds\n"
    "buffer_load_dwordx4 v1, s[8:11], s53 offen offset:2048 sc1 lds\n"
    "buffer_load_dwordx4 v1, s[8:11], s53 offen offset:3072 sc1 lds\n"
    "s_add_u32 s53, s65, 0x1000\n"
    "s_add_u32 m0, s25, 0x1000\n"
    "s_nop 0\n"
    "buffer_load_dwordx4 v1, s[8:11], s53 offen offset:0 sc1 lds\n"
    "buffer_load_dwordx4 v1, s[8:11], s53 offen offset:1024 sc1 lds\n"
    "; signal 4: every wave has its write-through stores acknowledged; barrier; one wave stores the flag line\n"
    "buffer_load_dwordx4 v1, s[8:11], s53 offen offset:2048 sc1 lds\n"
    "buffer_load_dwordx4 v1, s[8:11], s53 offen offset:3072 sc1 lds\n"
    "s_add_u32 s53, s65, 0x2000\n"
    "s_add_u32 m0, s25, 0x2000\n"
    "s_nop 0\n"
    "buffer_load_dwordx4 v1, s[8:11], s53 offen offset:0 sc1 lds\n"
    "s_waitcnt vmcnt(9)\n"
    "s_barrier\n"
    "s_cmp_lg_u32 s20, 0\n"
    "s_cbranch_scc1 Lskip_39_%=\n"
    "s_add_u32 s60, s39, 0x20000\n"
    "s_add_u32 s60, s60, s38\n"
    "s_mov_b64 exec, 0xff\n"
    "buffer_store_dwordx4 v[36:39], v1, s[8:11], s60 offen sc1\n"
    "s_mov_b64 exec, -1\n"
    "Lskip_39_%=:\n"
    "buffer_load_dwordx4 v1, s[8:11], s53 offen offset:1024 sc1 lds\n"
    "buffer_load_dwordx4 v1, s[8:11], s53 offen offset:2048 sc1 lds\n"
    "s_waitcnt vmcnt(9)\n"
    "s_barrier\n"
    "ds_read_b128 v[56:59], v6 offset:0\n"
    "ds_read_b128 v[60:63], v6 offset:20480\n"
    "ds_read_b128 v[64:67], v7 offset:0\n"
    "ds_read_b128 v[68:71], v7 offset:20480\n"
    "buffer_load_dwordx4 v1, s[8:11], s53 offen offset:3072 sc1 lds\n"
    "s_waitcnt lgkmcnt(3)\n"
    "v_mfma_f32_16x16x32_bf16 v[118:121], v[214:217], v[56:59], 0\n"
    "ds_read_b128 v[72:75], v6 offset:1024\n"
    "s_add_u32 s53, s65, 0x3000\n"
    "s_add_u32 m0, s25, 0x3000\n"
    "s_nop 0\n"
    "buffer_load_dwordx4 v1, s[8:11], s53 offen offset:0 sc1 lds\n"
    "s_waitcnt lgkmcnt(3)\n"
    "v_mfma_f32_16x16x32_bf16 v[122:125], v[214:217], v[60:63], 0\n"
    "ds_read_b128 v[76:79], v6 offset:21504\n"
    "s_waitcnt lgkmcnt(3)\n"
    "v_mfma_f32_16x16x32_bf16 v[126:129], v[214:217], v[64:67], 0\n"
    "ds_read_b128 v[80:83], v7 offset:1024\n"
    "s_waitcnt lgkmcnt(3)\n"
    "v_mfma_f32_16x16x32_bf16 v[130:133], v[214:217], v[68:71], 0\n"
    "ds_read_b128 v[84:87], v7 offset:21504\n"
    "buffer_load_dwordx4 v1, s[8:11], s53 offen offset:1024 sc1 lds\n"
    "s_waitcnt vmcnt(10)\n"
    "s_barrier\n"
    "s_waitcnt lgkmcnt(3)\n"
    "v_mfma_f32_16x16x32_bf16 v[118:121], v[218:221], v[72:75], v[118:121]\n"
    "ds_read_b128 v[56:59], v6 offset:2048\n"
    "s_waitcnt lgkmcnt(3)\n"
    "v_mfma_f32_16x16x32_bf16 v[122:125], v[218:221], v[76:79], v[122:125]\n"
    "ds_read_b128 v[60:63], v6 offset:22528\n"
    "buffer_load_dwordx4 v1, s[8:11], s53 offen offset:2048 sc1 lds\n"
    "s_waitcnt lgkmcnt(3)\n"
    "v_mfma_f32_16x16x32_bf16 v[126:129], v[218:221], v[80:83], v[126:129]\n"
    "ds_read_b128 v[64:67], v7 offset:2048\n"
    "s_waitcnt lgkmcnt(3)\n"
    "v_mfma_f32_16x16x32_bf16 v[130:133], v[218:221], v[84:87], v[130:133]\n"
    "ds_read_b128 v[68:71], v7 offset:22528\n"
    "s_waitcnt lgkmcnt(3)\n"
    "v_mfma_f32_16x16x32_bf16 v[118:121], v[222:225], v[56:59], v[118:121]\n"
    "ds_read_b128 v[72:75], v6 offset:3072\n"
    "buffer_load_dwordx4 v1, s[8:11], s53 offen offset:3072 sc1 lds\n"
    "s_waitcnt lgkmcnt(3)\n"
    "v_mfma_f32_16x16x32_bf16 v[122:125], v[222:225], v[60:63], v[122:125]\n"
    "ds_read_b128 v[76:79], v6 offset:23552\n"
    "s_waitcnt lgkmcnt(3)\n"
    "v_mfma_f32_16x16x32_bf16 v[126:129], v[222:225], v[64:67], v[126:129]\n"
    "ds_read_b128 v[80:83], v7 offset:3072\n"
    "s_waitcnt lgkmcnt(3)\n"
    "v_mfma_f32_16x16x32_bf16 v[130:133], v[222:225], v[68:71], v[130:133]\n"
    "ds_read_b128 v[84:87], v7 offset:23552\n"
    "s_add_u32 s53, s65, 0x4000\n"
    "s_add_u32 m0, s25, 0x4000\n"
    "s_nop 0\n"
    "buffer_load_dwordx4 v1, s[8:11], s53 offen offset:0 sc1 lds\n"
    "s_waitcnt vmcnt(9)\n"
    "s_barrier\n"
    "s_waitcnt lgkmcnt(3)\n"
    "v_mfma_f32_16x16x32_bf16 v[118:121], v[226:229], v[72:75], v[118:121]\n"
    "ds_read_b128 v[56:59], v6 offset:4096\n"
    "buffer_load_dwordx4 v1, s[8:11], s53 offen offset:1024 sc1 lds\n"
    "s_waitcnt lgkmcnt(3)\n"
    "v_mfma_f32_16x16x32_bf16 v[122:125], v[226:229], v[76:79], v[122:125]\n"
    "ds_read_b128 v[60:63], v6 offset:24576\n"
    "buffer_load_dwordx4 v1, s[8:11], s53 offen offset:2048 sc1 lds\n"
    "s_waitcnt lgkmcnt(3)\n"
    "v_mfma_f32_16x16x32_bf16 v[126:129], v[226:229], v[80:83], v[126:129]\n"
    "ds_read_b128 v[64:67], v7 offset:4096\n"
    "s_waitcnt lgkmcnt(3)\n"
    "v_mfma_f32_16x16x32_bf16 v[130:133], v[226:229], v[84:87], v[130:133]\n"
    "ds_read_b128 v[68:71], v7 offset:24576\n"
    "s_waitcnt lgkmcnt(3)\n"
    "v_mfma_f32_16x16x32_bf16 v[118:121], v[230:233], v[56:59], v[118:121]\n"
    "ds_read_b128 v[72:75], v6 offset:5120\n"
    "s_waitcnt lgkmcnt(3)\n"
    "v_mfma_f32_16x16x32_bf16 v[122:125], v[230:233], v[60:63], v[122:125]\n"
    "ds_read_b128 v[76:79], v6 offset:25600\n"
    "buffer_load_dwordx4 v1, s[8:11], s53 offen offset:3072 sc1 lds\n"
    "s_waitcnt lgkmcnt(3)\n"
    "v_mfma_f32_16x16x32_bf16 v[126:129], v[230:233], v[64:67], v[126:129]\n"
    "ds_read_b128 v[80:83], v7 offset:5120\n"
    "s_waitcnt lgkmcnt(3)\n"
    "v_mfma_f32_16x16x32_bf16 v[130:133], v[230:233], v[68:71], v[130:133]\n"
    "ds_read_b128 v[84:87], v7 offset:25600\n"
    "s_waitcnt lgkmcnt(3)\n"
    "v_mfma_f32_16x16x32_bf16 v[118:121], v[234:237], v[72:75], v[118:121]\n"
    "ds_read_b128 v[56:59], v6 offset:6144\n"
    "s_waitcnt lgkmcnt(3)\n"
    "v_mfma_f32_16x16x32_bf16 v[122:125], v[234:237], v[76:79], v[122:125]\n"
    "ds_read_b128 v[60:63], v6 offset:26624\n"
    "s_waitcnt lgkmcnt(3)\n"
    "v_mfma_f32_16x16x32_bf16 v[126:129], v[234:237], v[80:83], v[126:129]\n"
    "ds_read_b128 v[64:67], v7 offset:6144\n"
    "s_add_u32 s53, s65, 0x5000\n"
    "s_add_u32 m0, s25, 0x0\n"
    "s_nop 0\n"
    "buffer_load_dwordx4 v1, s[8:11], s53 offen offset:0 sc1 lds\n"
    "s_waitcnt lgkmcnt(3)\n"
    "v_mfma_f32_16x16x32_bf16 v[130:133], v[234:237], v[84:87], v[130:133]\n"
    "ds_read_b128 v[68:71], v7 offset:26624\n"
    "s_add_u32 s71, s38, 0x20000\n"
    "buffer_load_dwordx2 v[46:47], v5, s[8:11], s71 offen sc1\n"
    "buffer_load_dwordx4 v1, s[8:11], s53 offen offset:1024 sc1 lds\n"
    "s_waitcnt lgkmcnt(3)\n"
    "v_mfma_f32_16x16x32_bf16 v[118:121], v[238:241], v[56:59], v[118:121]\n"
    "ds_read_b128 v[72:75], v6 offset:7168\n"
    "s_waitcnt lgkmcnt(3)\n"
    "v_mfma_f32_16x16x32_bf16 v[122:125], v[238:241], v[60:63], v[122:125]\n"
    "ds_read_b128 v[76:79], v6 offset:27648\n"
    "s_waitcnt lgkmcnt(3)\n"
    "v_mfma_f32_16x16x32_bf16 v[126:129], v[238:241], v[64:67], v[126:129]\n"
    "ds_read_b128 v[80:83], v7 offset:7168\n"
    "s_waitcnt lgkmcnt(3)\n"
    "v_mfma_f32_16x16x32_bf16 v[130:133], v[238:241], v[68:71], v[130:133]\n"
    "ds_read_b128 v[84:87], v7 offset:27648\n"
    "buffer_load_dwordx4 v1, s[8:11], s53 offen offset:2048 sc1 lds\n"
    "s_waitcnt vmcnt(12)\n"
    "s_barrier\n"
    "s_waitcnt lgkmcnt(3)\n"
    "v_mfma_f32_16x16x32_bf16 v[118:121], v[242:245], v[72:75], v[118:121]\n"
    "ds_read_b128 v[56:59], v6 offset:8192\n"
    "buffer_load_dwordx4 v1, s[8:11], s53 offen offset:3072 sc1 lds\n"
    "s_waitcnt lgkmcnt(3)\n"
    "v_mfma_f32_16x16x32_bf16 v[122:125], v[242:245], v[76:79], v[122:125]\n"
    "ds_read_b128 v[60:63], v6 offset:28672\n"
    "s_waitcnt lgkmcnt(3)\n"
    "v_mfma_f32_16x16x32_bf16 v[126:129], v[242:245], v[80:83], v[126:129]\n"
    "ds_read_b128 v[64:67], v7 offset:8192\n"
    "s_waitcnt lgkmcnt(3)\n"
    "v_mfma_f32_16x16x32_bf16 v[130:133], v[242:245], v[84:87], v[130:133]\n"
    "ds_read_b128 v[68:71], v7 offset:28672\n"
    "s_waitcnt lgkmcnt(3)\n"
    "v_mfma_f32_16x16x32_bf16 v[118:121], v[246:249], v[56:59], v[118:121]\n"
    "ds_read_b128 v[72:75], v6 offset:9216\n"
    "s_add_u32 s53, s65, 0x6000\n"
    "s_add_u32 m0, s25, 0x1000\n"
    "s_nop 0\n"
    "buffer_load_dwordx4 v1, s[8:11], s53 offen offset:0 sc1 lds\n"
    "s_waitcnt lgkmcnt(3)\n"
    "v_mfma_f32_16x16x32_bf16 v[122:125], v[246:249], v[60:63], v[122:125]\n"
    "ds_read_b128 v[76:79], v6 offset:29696\n"
    "s_waitcnt lgkmcnt(3)\n"
    "v_mfma_f32_16x16x32_bf16 v[126:129], v[246:249], v[64:67], v[126:129]\n"
    "ds_read_b128 v[80:83], v7 offset:9216\n"
    "s_waitcnt lgkmcnt(3)\n"
    "v_mfma_f32_16x16x32_bf16 v[130:133], v[246:249], v[68:71], v[130:133]\n"
    "ds_read_b128 v[84:87], v7 offset:29696\n"
    "buffer_load_dwordx4 v1, s[8:11], s53 offen offset:1024 sc1 lds\n"
    "s_waitcnt lgkmcnt(3)\n"
    "v_mfma_f32_16x16x32_bf16 v[118:121], v[250:253], v[72:75], v[118:121]\n"
    "ds_read_b128 v[56:59], v6 offset:10240\n"
    "s_waitcnt lgkmcnt(3)\n"
    "v_mfma_f32_16x16x32_bf16 v[122:125], v[250:253], v[76:79], v[122:125]\n"
    "ds_read_b128 v[60:63], v6 offset:30720\n"
    "s_waitcnt lgkmcnt(3)\n"
    "v_mfma_f32_16x16x32_bf16 v[126:129], v[250:253], v[80:83], v[126:129]\n"
    "ds_read_b128 v[64:67], v7 offset:10240\n"
    "buffer_load_dwordx4 v1, s[8:11], s53 offen offset:2048 sc1 lds\n"
    "s_waitcnt lgkmcnt(3)\n"
    "v_mfma_f32_16x16x32_bf16 v[130:133], v[250:253], v[84:87], v[130:133]\n"
    "ds_read_b128 v[68:71], v7 offset:30720\n"
    "s_waitcnt lgkmcnt(3)\n"
    "v_mfma_f32_16x16x32_bf16 v[118:121], a[200:203], v[56:59], v[118:121]\n"
    "ds_read_b128 v[72:75], v6 offset:11264\n"
    "s_waitcnt lgkmcnt(3)\n"
    "v_mfma_f32_16x16x32_bf16 v[122:125], a[200:203], v[60:63], v[122:125]\n"
    "ds_read_b128 v[76:79], v6 offset:31744\n"
    "s_waitcnt lgkmcnt(3)\n"
    "v_mfma_f32_16x16x32_bf16 v[126:129], a[200:203], v[64:67], v[126:129]\n"
    "ds_read_b128 v[80:83], v7 offset:11264\n"
    "buffer_load_dwordx4 a[0:3], v1, s[8:11], s28 offen offset:0 sc1\n"
    "s_waitcnt lgkmcnt(3)\n"
    "v_mfma_f32_16x16x32_bf16 v[130:133], a[200:203], v[68:71], v[130:133]\n"
    "ds_read_b128 v[84:87], v7 offset:31744\n"
    "s_waitcnt vmcnt(13)\n"
    "s_barrier\n"
    "s_waitcnt lgkmcnt(3)\n"
    "v_mfma_f32_16x16x32_bf16 v[118:121], a[204:207], v[72:75], v[118:121]\n"
    "ds_read_b128 v[56:59], v6 offset:12288\n"
    "buffer_load_dwordx4 v1, s[8:11], s53 offen offset:3072 sc1 lds\n"
    "s_waitcnt lgkmcnt(3)\n"
    "v_mfma_f32_16x16x32_bf16 v[122:125], a[204:207], v[76:79], v[122:125]\n"
    "ds_read_b128 v[60:63], v6 offset:32768\n"
    "s_waitcnt lgkmcnt(3)\n"
    "v_mfma_f32_16x16x32_bf16 v[126:129], a[204:207], v[80:83], v[126:129]\n"
    "ds_read_b128 v[64:67], v7 offset:12288\n"
    "s_waitcnt lgkmcnt(3)\n"
    "v_mfma_f32_16x16x32_bf16 v[130:133], a[204:207], v[84:87], v[130:133]\n"
    "ds_read_b128 v[68:71], v7 offset:32768\n"
    "s_add_u32 s53, s65, 0x7000\n"
    "s_add_u32 m0, s25, 0x2000\n"
    "s_nop 0\n"
    "buffer_load_dwordx4 v1, s[8:11], s53 offen offset:0 sc1 lds\n"
    "s_waitcnt lgkmcnt(3)\n"
    "v_mfma_f32_16x16x32_bf16 v[118:121], a[208:211], v[56:59], v[118:121]\n"
    "ds_read_b128 v[72:75], v6 offset:13312\n"
    "s_waitcnt lgkmcnt(3)\n"
    "v_mfma_f32_16x16x32_bf16 v[122:125], a[208:211], v[60:63], v[122:125]\n"
    "ds_read_b128 v[76:79], v6 offset:33792\n"
    "s_waitcnt lgkmcnt(3)\n"
    "v_mfma_f32_16x16x32_bf16 v[126:129], a[208:211], v[64:67], v[126:129]\n"
    "ds_read_b128 v[80:83], v7 offset:13312\n"
    "s_waitcnt lgkmcnt(3)\n"
    "v_mfma_f32_16x16x32_bf16 v[130:133], a[208:211], v[68:71], v[130:133]\n"
    "ds_read_b128 v[84:87], v7 offset:33792\n"
    "s_waitcnt lgkmcnt(3)\n"
    "v_mfma_f32_16x16x32_bf16 v[118:121], a[212:215], v[72:75], v[118:121]\n"
    "ds_read_b128 v[56:59], v6 offset:14336\n"
    "buffer_load_dwordx4 v1, s[8:11], s53 offen offset:1024 sc1 lds\n"
    "s_waitcnt lgkmcnt(3)\n"
    "v_mfma_f32_16x16x32_bf16 v[122:125], a[212:215], v[76:79], v[122:125]\n"
    "ds_read_b128 v[60:63], v6 offset:34816\n"
    "s_waitcnt lgkmcnt(3)\n"
    "v_mfma_f32_16x16x32_bf16 v[126:129], a[212:215], v[80:83], v[126:129]\n"
    "ds_read_b128 v[64:67], v7 offset:14336\n"
    "s_waitcnt lgkmcnt(3)\n"
    "v_mfma_f32_16x16x32_bf16 v[130:133], a[212:215], v[84:87], v[130:133]\n"
    "ds_read_b128 v[68:71], v7 offset:34816\n"
    "s_waitcnt lgkmcnt(3)\n"
    "v_mfma_f32_16x16x32_bf16 v[118:121], a[216:219], v[56:59], v[118:121]\n"
    "ds_read_b128 v[72:75], v6 offset:15360\n"
    "buffer_load_dwordx4 v1, s[8:11], s53 offen offset:2048 sc1 lds\n"
    "s_waitcnt lgkmcnt(3)\n"
    "v_mfma_f32_16x16x32_bf16 v[122:125], a[216:219], v[60:63], v[122:125]\n"
    "ds_read_b128 v[76:79], v6 offset:35840\n"
    "s_waitcnt lgkmcnt(3)\n"
    "v_mfma_f32_16x16x32_bf16 v[126:129], a[216:219], v[64:67], v[126:129]\n"
    "ds_read_b128 v[80:83], v7 offset:15360\n"
    "s_waitcnt lgkmcnt(3)\n"
    "v_mfma_f32_16x16x32_bf16 v[130:133], a[216:219], v[68:71], v[130:133]\n"
    "ds_read_b128 v[84:87], v7 offset:35840\n"
    "buffer_load_dwordx4 a[4:7], v1, s[8:11], s28 offen offset:1024 sc1\n"
    "s_waitcnt vmcnt(14)\n"
    "s_barrier\n"
    "s_waitcnt lgkmcnt(3)\n"
    "v_mfma_f32_16x16x32_bf16 v[118:121], a[220:223], v[72:75], v[118:121]\n"
    "ds_read_b128 v[56:59], v6 offset:16384\n"
    "s_waitcnt lgkmcnt(3)\n"
    "v_mfma_f32_16x16x32_bf16 v[122:125], a[220:223], v[76:79], v[122:125]\n"
    "ds_read_b128 v[60:63], v6 offset:36864\n"
    "buffer_load_dwordx4 v1, s[8:11], s53 offen offset:3072 sc1 lds\n"
    "s_waitcnt lgkmcnt(3)\n"
    "v_mfma_f32_16x16x32_bf16 v[126:129], a[220:223], v[80:83], v[126:129]\n"
    "ds_read_b128 v[64:67], v7 offset:16384\n"
    "s_waitcnt lgkmcnt(3)\n"
    "v_mfma_f32_16x16x32_bf16 v[130:133], a[220:223], v[84:87], v[130:133]\n"
    "ds_read_b128 v[68:71], v7 offset:36864\n"
    "s_waitcnt lgkmcnt(3)\n"
    "v_mfma_f32_16x16x32_bf16 v[118:121], a[224:227], v[56:59], v[118:121]\n"
    "ds_read_b128 v[72:75], v6 offset:17408\n"
    "buffer_load_dwordx4 a[8:11], v1, s[8:11], s28 offen offset:2048 sc1\n"
    "s_waitcnt lgkmcnt(3)\n"
    "v_mfma_f32_16x16x32_bf16 v[122:125], a[224:227], v[60:63], v[122:125]\n"
    "ds_read_b128 v[76:79], v6 offset:37888\n"
    "s_waitcnt vmcnt(14)\n"
    "v_cmp_ne_u64 vcc, s[26:27], v[46:47]\n"
    "s_nop 0\n"
    "s_cbranch_vccnz Lspin_40_%=\n"
    "Lgo_41_%=:\n"
    "s_waitcnt lgkmcnt(3)\n"
    "v_mfma_f32_16x16x32_bf16 v[126:129], a[224:227], v[64:67], v[126:129]\n"
    "ds_read_b128 v[80:83], v7 offset:17408\n"
    "s_waitcnt lgkmcnt(3)\n"
    "v_mfma_f32_16x16x32_bf16 v[130:133], a[224:227], v[68:71], v[130:133]\n"
    "ds_read_b128 v[84:87], v7 offset:37888\n"
    "s_add_u32 s53, s65, 0x8000\n"
    "s_add_u32 m0, s25, 0x3000\n"
    "s_nop 0\n"
    "buffer_load_dwordx4 v1, s[8:11], s53 offen offset:0 sc1 lds\n"
    "s_waitcnt lgkmcnt(3)\n"
    "v_mfma_f32_16x16x32_bf16 v[118:121], a[228:231], v[72:75], v[118:121]\n"
    "ds_read_b128 v[56:59], v6 offset:18432\n"
    "s_waitcnt lgkmcnt(3)\n"
    "v_mfma_f32_16x16x32_bf16 v[122:125], a[228:231], v[76:79], v[122:125]\n"
    "ds_read_b128 v[60:63], v6 offset:38912\n"
    "s_waitcnt lgkmcnt(3)\n"
    "v_mfma_f32_16x16x32_bf16 v[126:129], a[228:231], v[80:83], v[126:129]\n"
    "ds_read_b128 v[64:67], v7 offset:18432\n"
    "buffer_load_dwordx4 v1, s[8:11], s53 offen offset:1024 sc1 lds\n"
    "s_waitcnt lgkmcnt(3)\n"
    "v_mfma_f32_16x16x32_bf16 v[130:133], a[228:231], v[84:87], v[130:133]\n"
    "ds_read_b128 v[68:71], v7 offset:38912\n"
    "s_waitcnt lgkmcnt(3)\n"
    "v_mfma_f32_16x16x32_bf16 v[118:121], a[232:235], v[56:59], v[118:121]\n"
    "ds_read_b128 v[72:75], v6 offset:19456\n"
    "s_waitcnt lgkmcnt(3)\n"
    "v_mfma_f32_16x16x32_bf16 v[122:125], a[232:235], v[60:63], v[122:125]\n"
    "ds_read_b128 v[76:79], v6 offset:39936\n"
    "s_waitcnt lgkmcnt(3)\n"
    "v_mfma_f32_16x16x32_bf16 v[126:129], a[232:235], v[64:67], v[126:129]\n"
    "ds_read_b128 v[80:83], v7 offset:19456\n"
    "buffer_load_dwordx4 v1, s[8:11], s53 offen offset:2048 sc1 lds\n"
    "s_waitcnt lgkmcnt(3)\n"
    "v_mfma_f32_16x16x32_bf16 v[130:133], a[232:235], v[68:71], v[130:133]\n"
    "ds_read_b128 v[84:87], v7 offset:39936\n"
    "s_waitcnt vmcnt(14)\n"
    "s_barrier\n"
    "s_waitcnt lgkmcnt(3)\n"
    "v_mfma_f32_16x16x32_bf16 v[118:121], a[236:239], v[72:75], v[118:121]\n"
    "ds_read_b128 v[56:59], v6 offset:0\n"
    "buffer_load_dwordx4 v1, s[8:11], s53 offen offset:3072 sc1 lds\n"
    "s_waitcnt lgkmcnt(3)\n"
    "v_mfma_f32_16x16x32_bf16 v[122:125], a[236:239], v[76:79], v[122:125]\n"
    "ds_read_b128 v[60:63], v6 offset:20480\n"
    "s_waitcnt lgkmcnt(3)\n"
    "v_mfma_f32_16x16x32_bf16 v[126:129], a[236:239], v[80:83], v[126:129]\n"
    "ds_read_b128 v[64:67], v7 offset:0\n"
    "s_waitcnt lgkmcnt(3)\n"
    "v_mfma_f32_16x16x32_bf16 v[130:133], a[236:239], v[84:87], v[130:133]\n"
    "ds_read_b128 v[68:71], v7 offset:20480\n"
    "s_add_u32 s53, s65, 0x9000\n"
    "s_add_u32 m0, s25, 0x4000\n"
    "s_nop 0\n"
    "buffer_load_dwordx4 v1, s[8:11], s53 offen offset:0 sc1 lds\n"
    "s_waitcnt lgkmcnt(3)\n"
    "v_mfma_f32_16x16x32_bf16 v[118:121], a[240:243], v[56:59], v[118:121]\n"
    "ds_read_b128 v[72:75], v6 offset:1024\n"
    "s_waitcnt lgkmcnt(3)\n"
    "v_mfma_f32_16x16x32_bf16 v[122:125], a[240:243], v[60:63], v[122:125]\n"
    "ds_read_b128 v[76:79], v6 offset:21504\n"
    "s_waitcnt lgkmcnt(3)\n"
    "v_mfma_f32_16x16x32_bf16 v[126:129], a[240:243], v[64:67], v[126:129]\n"
    "ds_read_b128 v[80:83], v7 offset:1024\n"
    "buffer_load_dwordx4 v1, s[8:11], s53 offen offset:1024 sc1 lds\n"
    "s_waitcnt lgkmcnt(3)\n"
    "v_mfma_f32_16x16x32_bf16 v[130:133], a[240:243], v[68:71], v[130:133]\n"
    "ds_read_b128 v[84:87], v7 offset:21504\n"
    "s_waitcnt lgkmcnt(3)\n"
    "v_mfma_f32_16x16x32_bf16 v[118:121], a[244:247], v[72:75], v[118:121]\n"
    "ds_read_b128 v[56:59], v6 offset:2048\n"
    "s_waitcnt lgkmcnt(3)\n"
    "v_mfma_f32_16x16x32_bf16 v[122:125], a[244:247], v[76:79], v[122:125]\n"
    "ds_read_b128 v[60:63], v6 offset:22528\n"
    "s_waitcnt lgkmcnt(3)\n"
    "v_mfma_f32_16x16x32_bf16 v[126:129], a[244:247], v[80:83], v[126:129]\n"
    "ds_read_b128 v[64:67], v7 offset:2048\n"
    "buffer_load_dwordx4 v1, s[8:11], s53 offen offset:2048 sc1 lds\n"
    "s_waitcnt lgkmcnt(3)\n"
    "v_mfma_f32_16x16x32_bf16 v[130:133], a[244:247], v[84:87], v[130:133]\n"
    "ds_read_b128 v[68:71], v7 offset:22528\n"
    "s_waitcnt lgkmcnt(3)\n"
    "v_mfma_f32_16x16x32_bf16 v[118:121], a[248:251], v[56:59], v[118:121]\n"
    "ds_read_b128 v[72:75], v6 offset:3072\n"
    "s_waitcnt lgkmcnt(3)\n"
    "v_mfma_f32_16x16x32_bf16 v[122:125], a[248:251], v[60:63], v[122:125]\n"
    "ds_read_b128 v[76:79], v6 offset:23552\n"
    "buffer_load_dwordx4 a[12:15], v1, s[8:11], s28 offen offset:3072 sc1\n"
    "s_waitcnt lgkmcnt(3)\n"
    "v_mfma_f32_16x16x32_bf16 v[126:129], a[248:251], v[64:67], v[126:129]\n"
    "ds_read_b128 v[80:83], v7 offset:3072\n"
    "s_waitcnt lgkmcnt(3)\n"
    "v_mfma_f32_16x16x32_bf16 v[130:133], a[248:251], v[68:71], v[130:133]\n"
    "ds_read_b128 v[84:87], v7 offset:23552\n"
    "s_waitcnt vmcnt(14)\n"
    "s_barrier\n"
    "s_waitcnt lgkmcnt(3)\n"
    "v_mfma_f32_16x16x32_bf16 v[118:121], a[252:255], v[72:75], v[118:121]\n"
    "ds_read_b128 v[56:59], v6 offset:4096\n"
    "buffer_load_dwordx4 v1, s[8:11], s53 offen offset:3072 sc1 lds\n"
    "s_waitcnt lgkmcnt(3)\n"
    "v_mfma_f32_16x16x32_bf16 v[122:125], a[252:255], v[76:79], v[122:125]\n"
    "ds_read_b128 v[60:63], v6 offset:24576\n"
    "s_waitcnt lgkmcnt(3)\n"
    "v_mfma_f32_16x16x32_bf16 v[126:129], a[252:255], v[80:83], v[126:129]\n"
    "ds_read_b128 v[64:67], v7 offset:4096\n"
    "s_add_u32 s53, s65, 0xa000\n"
    "s_add_u32 m0, s25, 0x0\n"
    "s_nop 0\n"
    "buffer_load_dwordx4 v1, s[8:11], s53 offen offset:0 sc1 lds\n"
    "s_waitcnt lgkmcnt(3)\n"
    "v_mfma_f32_16x16x32_bf16 v[130:133], a[252:255], v[84:87], v[130:133]\n"
    "ds_read_b128 v[68:71], v7 offset:24576\n"
    "s_waitcnt lgkmcnt(3)\n"
    "v_mfma_f32_16x16x32_bf16 v[118:121], a[32:35], v[56:59], v[118:121]\n"
    "ds_read_b128 v[72:75], v6 offset:5120\n"
    "s_waitcnt lgkmcnt(3)\n"
    "v_mfma_f32_16x16x32_bf16 v[122:125], a[32:35], v[60:63], v[122:125]\n"
    "ds_read_b128 v[76:79], v6 offset:25600\n"
    "buffer_load_dwordx4 v1, s[8:11], s53 offen offset:1024 sc1 lds\n"
    "s_waitcnt lgkmcnt(3)\n"
    "v_mfma_f32_16x16x32_bf16 v[126:129], a[32:35], v[64:67], v[126:129]\n"
    "ds_read_b128 v[80:83], v7 offset:5120\n"
    "s_waitcnt lgkmcnt(3)\n"
    "v_mfma_f32_16x16x32_bf16 v[130:133], a[32:35], v[68:71], v[130:133]\n"
    "ds_read_b128 v[84:87], v7 offset:25600\n"
    "s_waitcnt lgkmcnt(3)\n"
    "v_mfma_f32_16x16x32_bf16 v[118:121], a[36:39], v[72:75], v[118:121]\n"
    "ds_read_b128 v[56:59], v6 offset:6144\n"
    "s_waitcnt lgkmcnt(3)\n"
    "v_mfma_f32_16x16x32_bf16 v[122:125], a[36:39], v[76:79], v[122:125]\n"
    "ds_read_b128 v[60:63], v6 offset:26624\n"
    "buffer_load_dwordx4 v1, s[8:11], s53 offen offset:2048 sc1 lds\n"
    "s_waitcnt lgkmcnt(3)\n"
    "v_mfma_f32_16x16x32_bf16 v[126:129], a[36:39], v[80:83], v[126:129]\n"
    "ds_read_b128 v[64:67], v7 offset:6144\n"
    "s_waitcnt lgkmcnt(3)\n"
    "v_mfma_f32_16x16x32_bf16 v[130:133], a[36:39], v[84:87], v[130:133]\n"
    "ds_read_b128 v[68:71], v7 offset:26624\n"
    "s_waitcnt lgkmcnt(3)\n"
    "v_mfma_f32_16x16x32_bf16 v[118:121], a[40:43], v[56:59], v[118:121]\n"
    "ds_read_b128 v[72:75], v6 offset:7168\n"
    "s_add_u32 s52, s28, 0x1000\n"
    "buffer_load_dwordx4 a[16:19], v1, s[8:11], s52 offen offset:0 sc1\n"
    "s_waitcnt lgkmcnt(3)\n"
    "v_mfma_f32_16x16x32_bf16 v[122:125], a[40:43], v[60:63], v[122:125]\n"
    "ds_read_b128 v[76:79], v6 offset:27648\n"
    "s_waitcnt lgkmcnt(3)\n"
    "v_mfma_f32_16x16x32_bf16 v[126:129], a[40:43], v[64:67], v[126:129]\n"
    "ds_read_b128 v[80:83], v7 offset:7168\n"
    "s_waitcnt lgkmcnt(3)\n"
    "v_mfma_f32_16x16x32_bf16 v[130:133], a[40:43], v[68:71], v[130:133]\n"
    "ds_read_b128 v[84:87], v7 offset:27648\n"
    "s_waitcnt vmcnt(14)\n"
    "s_barrier\n"
    "s_waitcnt lgkmcnt(3)\n"
    "v_mfma_f32_16x16x32_bf16 v[118:121], a[44:47], v[72:75], v[118:121]\n"
    "ds_read_b128 v[56:59], v6 offset:8192\n"
    "buffer_load_dwordx4 v1, s[8:11], s53 offen offset:3072 sc1 lds\n"
    "s_waitcnt lgkmcnt(3)\n"
    "v_mfma_f32_16x16x32_bf16 v[122:125], a[44:47], v[76:79], v[122:125]\n"
    "ds_read_b128 v[60:63], v6 offset:28672\n"
    "s_add_u32 s53, s65, 0xb000\n"
    "s_add_u32 m0, s25, 0x1000\n"
    "s_nop 0\n"
    "buffer_load_dwordx4 v1, s[8:11], s53 offen offset:0 sc1 lds\n"
    "s_waitcnt lgkmcnt(3)\n"
    "v_mfma_f32_16x16x32_bf16 v[126:129], a[44:47], v[80:83], v[126:129]\n"
    "ds_read_b128 v[64:67], v7 offset:8192\n"
    "s_waitcnt lgkmcnt(3)\n"
    "v_mfma_f32_16x16x32_bf16 v[130:133], a[44:47], v[84:87], v[130:133]\n"
    "ds_read_b128 v[68:71], v7 offset:28672\n"
    "s_waitcnt lgkmcnt(3)\n"
    "v_mfma_f32_16x16x32_bf16 v[118:121], a[48:51], v[56:59], v[118:121]\n"
    "ds_read_b128 v[72:75], v6 offset:9216\n"
    "buffer_load_dwordx4 v1, s[8:11], s53 offen offset:1024 sc1 lds\n"
    "s_waitcnt lgkmcnt(3)\n"
    "v_mfma_f32_16x16x32_bf16 v[122:125], a[48:51], v[60:63], v[122:125]\n"
    "ds_read_b128 v[76:79], v6 offset:29696\n"
    "s_waitcnt lgkmcnt(3)\n"
    "v_mfma_f32_16x16x32_bf16 v[126:129], a[48:51], v[64:67], v[126:129]\n"
    "ds_read_b128 v[80:83], v7 offset:9216\n"
    "s_waitcnt lgkmcnt(3)\n"
    "v_mfma_f32_16x16x32_bf16 v[130:133], a[48:51], v[68:71], v[130:133]\n"
    "ds_read_b128 v[84:87], v7 offset:29696\n"
    "s_waitcnt lgkmcnt(3)\n"
    "v_mfma_f32_16x16x32_bf16 v[118:121], a[52:55], v[72:75], v[118:121]\n"
    "ds_read_b128 v[56:59], v6 offset:10240\n"
    "buffer_load_dwordx4 v1, s[8:11], s53 offen offset:2048 sc1 lds\n"
    "s_waitcnt lgkmcnt(3)\n"
    "v_mfma_f32_16x16x32_bf16 v[122:125], a[52:55], v[76:79], v[122:125]\n"
    "ds_read_b128 v[60:63], v6 offset:30720\n"
    "s_waitcnt lgkmcnt(3)\n"
    "v_mfma_f32_16x16x32_bf16 v[126:129], a[52:55], v[80:83], v[126:129]\n"
    "ds_read_b128 v[64:67], v7 offset:10240\n"
    "s_waitcnt lgkmcnt(3)\n"
    "v_mfma_f32_16x16x32_bf16 v[130:133], a[52:55], v[84:87], v[130:133]\n"
    "ds_read_b128 v[68:71], v7 offset:30720\n"
    "s_waitcnt lgkmcnt(3)\n"
    "v_mfma_f32_16x16x32_bf16 v[118:121], a[56:59], v[56:59], v[118:121]\n"
    "ds_read_b128 v[72:75], v6 offset:11264\n"
    "buffer_load_dwordx4 a[20:23], v1, s[8:11], s52 offen offset:1024 sc1\n"
    "s_waitcnt lgkmcnt(3)\n"
    "v_mfma_f32_16x16x32_bf16 v[122:125], a[56:59], v[60:63], v[122:125]\n"
    "ds_read_b128 v[76:79], v6 offset:31744\n"
    "s_waitcnt lgkmcnt(3)\n"
    "v_mfma_f32_16x16x32_bf16 v[126:129], a[56:59], v[64:67], v[126:129]\n"
    "ds_read_b128 v[80:83], v7 offset:11264\n"
    "s_waitcnt lgkmcnt(3)\n"
    "v_mfma_f32_16x16x32_bf16 v[130:133], a[56:59], v[68:71], v[130:133]\n"
    "ds_read_b128 v[84:87], v7 offset:31744\n"
    "buffer_load_dwordx4 a[24:27], v1, s[8:11], s52 offen offset:2048 sc1\n"
    "s_waitcnt vmcnt(15)\n"
    "s_barrier\n"
    "s_waitcnt lgkmcnt(3)\n"
    "v_mfma_f32_16x16x32_bf16 v[118:121], a[60:63], v[72:75], v[118:121]\n"
    "ds_read_b128 v[56:59], v6 offset:12288\n"
    "s_waitcnt lgkmcnt(3)\n"
    "v_mfma_f32_16x16x32_bf16 v[122:125], a[60:63], v[76:79], v[122:125]\n"
    "ds_read_b128 v[60:63], v6 offset:32768\n"
    "buffer_load_dwordx4 v1, s[8:11], s53 offen offset:3072 sc1 lds\n"
    "s_waitcnt lgkmcnt(3)\n"
    "v_mfma_f32_16x16x32_bf16 v[126:129], a[60:63], v[80:83], v[126:129]\n"
    "ds_read_b128 v[64:67], v7 offset:12288\n"
    "s_waitcnt lgkmcnt(3)\n"
    "v_mfma_f32_16x16x32_bf16 v[130:133], a[60:63], v[84:87], v[130:133]\n"
    "ds_read_b128 v[68:71], v7 offset:32768\n"
    "s_waitcnt lgkmcnt(3)\n"
    "v_mfma_f32_16x16x32_bf16 v[118:121], a[64:67], v[56:59], v[118:121]\n"
    "ds_read_b128 v[72:75], v6 offset:13312\n"
    "s_add_u32 s53, s65, 0xc000\n"
    "s_add_u32 m0, s25, 0x2000\n"
    "s_nop 0\n"
    "buffer_load_dwordx4 v1, s[8:11], s53 offen offset:0 sc1 lds\n"
    "s_waitcnt lgkmcnt(3)\n"
    "v_mfma_f32_16x16x32_bf16 v[122:125], a[64:67], v[60:63], v[122:125]\n"
    "ds_read_b128 v[76:79], v6 offset:33792\n"
    "s_waitcnt lgkmcnt(3)\n"
    "v_mfma_f32_16x16x32_bf16 v[126:129], a[64:67], v[64:67], v[126:129]\n"
    "ds_read_b128 v[80:83], v7 offset:13312\n"
    "s_waitcnt lgkmcnt(3)\n"
    "v_mfma_f32_16x16x32_bf16 v[130:133], a[64:67], v[68:71], v[130:133]\n"
    "ds_read_b128 v[84:87], v7 offset:33792\n"
    "buffer_load_dwordx4 v1, s[8:11], s53 offen offset:1024 sc1 lds\n"
    "s_waitcnt lgkmcnt(3)\n"
    "v_mfma_f32_16x16x32_bf16 v[118:121], a[68:71], v[72:75], v[118:121]\n"
    "ds_read_b128 v[56:59], v6 offset:14336\n"
    "s_waitcnt lgkmcnt(3)\n"
    "v_mfma_f32_16x16x32_bf16 v[122:125], a[68:71], v[76:79], v[122:125]\n"
    "ds_read_b128 v[60:63], v6 offset:34816\n"
    "s_waitcnt lgkmcnt(3)\n"
    "v_mfma_f32_16x16x32_bf16 v[126:129], a[68:71], v[80:83], v[126:129]\n"
    "ds_read_b128 v[64:67], v7 offset:14336\n"
    "s_waitcnt lgkmcnt(3)\n"
    "v_mfma_f32_16x16x32_bf16 v[130:133], a[68:71], v[84:87], v[130:133]\n"
    "ds_read_b128 v[68:71], v7 offset:34816\n"
    "s_waitcnt lgkmcnt(3)\n"
    "v_mfma_f32_16x16x32_bf16 v[118:121], a[72:75], v[56:59], v[118:121]\n"
    "ds_read_b128 v[72:75], v6 offset:15360\n"
    "buffer_load_dwordx4 v1, s[8:11], s53 offen offset:2048 sc1 lds\n"
    "s_waitcnt lgkmcnt(3)\n"
    "v_mfma_f32_16x16x32_bf16 v[122:125], a[72:75], v[60:63], v[122:125]\n"
    "ds_read_b128 v[76:79], v6 offset:35840\n"
    "s_waitcnt lgkmcnt(3)\n"
    "v_mfma_f32_16x16x32_bf16 v[126:129], a[72:75], v[64:67], v[126:129]\n"
    "ds_read_b128 v[80:83], v7 offset:15360\n"
    "s_waitcnt lgkmcnt(3)\n"
    "v_mfma_f32_16x16x32_bf16 v[130:133], a[72:75], v[68:71], v[130:133]\n"
    "ds_read_b128 v[84:87], v7 offset:35840\n"
    "buffer_load_dwordx4 a[28:31], v1, s[8:11], s52 offen offset:3072 sc1\n"
    "s_waitcnt vmcnt(15)\n"
    "s_barrier\n"
    "s_waitcnt lgkmcnt(3)\n"
    "v_mfma_f32_16x16x32_bf16 v[118:121], a[76:79], v[72:75], v[118:121]\n"
    "ds_read_b128 v[56:59], v6 offset:16384\n"
    "s_waitcnt lgkmcnt(3)\n"
    "v_mfma_f32_16x16x32_bf16 v[122:125], a[76:79], v[76:79], v[122:125]\n"
    "ds_read_b128 v[60:63], v6 offset:36864\n"
    "buffer_load_dwordx4 v1, s[8:11], s53 offen offset:3072 sc1 lds\n"
    "s_waitcnt lgkmcnt(3)\n"
    "v_mfma_f32_16x16x32_bf16 v[126:129], a[76:79], v[80:83], v[126:129]\n"
    "ds_read_b128 v[64:67], v7 offset:16384\n"
    "s_waitcnt lgkmcnt(3)\n"
    "v_mfma_f32_16x16x32_bf16 v[130:133], a[76:79], v[84:87], v[130:133]\n"
    "ds_read_b128 v[68:71], v7 offset:36864\n"
    "s_waitcnt lgkmcnt(3)\n"
    "v_mfma_f32_16x16x32_bf16 v[118:121], a[80:83], v[56:59], v[118:121]\n"
    "ds_read_b128 v[72:75], v6 offset:17408\n"
    "s_add_u32 s53, s65, 0xd000\n"
    "s_add_u32 m0, s25, 0x3000\n"
    "s_nop 0\n"
    "buffer_load_dwordx4 v1, s[8:11], s53 offen offset:0 sc1 lds\n"
    "s_waitcnt lgkmcnt(3)\n"
    "v_mfma_f32_16x16x32_bf16 v[122:125], a[80:83], v[60:63], v[122:125]\n"
    "ds_read_b128 v[76:79], v6 offset:37888\n"
    "s_waitcnt lgkmcnt(3)\n"
    "v_mfma_f32_16x16x32_bf16 v[126:129], a[80:83], v[64:67], v[126:129]\n"
    "ds_read_b128 v[80:83], v7 offset:17408\n"
    "s_waitcnt lgkmcnt(3)\n"
    "v_mfma_f32_16x16x32_bf16 v[130:133], a[80:83], v[68:71], v[130:133]\n"
    "ds_read_b128 v[84:87], v7 offset:37888\n"
    "buffer_load_dwordx4 v1, s[8:11], s53 offen offset:1024 sc1 lds\n"
    "s_waitcnt lgkmcnt(3)\n"
    "v_mfma_f32_16x16x32_bf16 v[118:121], a[84:87], v[72:75], v[118:121]\n"
    "ds_read_b128 v[56:59], v6 offset:18432\n"
    "s_waitcnt lgkmcnt(3)\n"
    "v_mfma_f32_16x16x32_bf16 v[122:125], a[84:87], v[76:79], v[122:125]\n"
    "ds_read_b128 v[60:63], v6 offset:38912\n"
    "s_waitcnt lgkmcnt(3)\n"
    "v_mfma_f32_16x16x32_bf16 v[126:129], a[84:87], v[80:83], v[126:129]\n"
    "ds_read_b128 v[64:67], v7 offset:18432\n"
    "s_waitcnt lgkmcnt(3)\n"
    "v_mfma_f32_16x16x32_bf16 v[130:133], a[84:87], v[84:87], v[130:133]\n"
    "ds_read_b128 v[68:71], v7 offset:38912\n"
    "buffer_load_dwordx4 v1, s[8:11], s53 offen offset:2048 sc1 lds\n"
    "s_waitcnt lgkmcnt(3)\n"
    "v_mfma_f32_16x16x32_bf16 v[118:121], a[88:91], v[56:59], v[118:121]\n"
    "ds_read_b128 v[72:75], v6 offset:19456\n"
    "s_waitcnt lgkmcnt(3)\n"
    "v_mfma_f32_16x16x32_bf16 v[122:125], a[88:91], v[60:63], v[122:125]\n"
    "ds_read_b128 v[76:79], v6 offset:39936\n"
    "s_waitcnt lgkmcnt(3)\n"
    "v_mfma_f32_16x16x32_bf16 v[126:129], a[88:91], v[64:67], v[126:129]\n"
    "ds_read_b128 v[80:83], v7 offset:19456\n"
    "s_add_u32 s52, s28, 0x2000\n"
    "buffer_load_dwordx4 a[32:35], v1, s[8:11], s52 offen offset:0 sc1\n"
    "s_waitcnt lgkmcnt(3)\n"
    "v_mfma_f32_16x16x32_bf16 v[130:133], a[88:91], v[68:71], v[130:133]\n"
    "ds_read_b128 v[84:87], v7 offset:39936\n"
    "s_waitcnt vmcnt(15)\n"
    "s_barrier\n"
    "s_waitcnt lgkmcnt(3)\n"
    "v_mfma_f32_16x16x32_bf16 v[118:121], a[92:95], v[72:75], v[118:121]\n"
    "ds_read_b128 v[56:59], v6 offset:0\n"
    "buffer_load_dwordx4 v1, s[8:11], s53 offen offset:3072 sc1 lds\n"
    "s_waitcnt lgkmcnt(3)\n"
    "v_mfma_f32_16x16x32_bf16 v[122:125], a[92:95], v[76:79], v[122:125]\n"
    "ds_read_b128 v[60:63], v6 offset:20480\n"
    "s_waitcnt lgkmcnt(3)\n"
    "v_mfma_f32_16x16x32_bf16 v[126:129], a[92:95], v[80:83], v[126:129]\n"
    "ds_read_b128 v[64:67], v7 offset:0\n"
    "s_waitcnt lgkmcnt(3)\n"
    "v_mfma_f32_16x16x32_bf16 v[130:133], a[92:95], v[84:87], v[130:133]\n"
    "ds_read_b128 v[68:71], v7 offset:20480\n"
    "buffer_load_dwordx4 a[36:39], v1, s[8:11], s52 offen offset:1024 sc1\n"
    "s_waitcnt lgkmcnt(3)\n"
    "v_mfma_f32_16x16x32_bf16 v[118:121], a[96:99], v[56:59], v[118:121]\n"
    "ds_read_b128 v[72:75], v6 offset:1024\n"
    "s_waitcnt lgkmcnt(3)\n"
    "v_mfma_f32_16x16x32_bf16 v[122:125], a[96:99], v[60:63], v[122:125]\n"
    "ds_read_b128 v[76:79], v6 offset:21504\n"
    "s_waitcnt lgkmcnt(3)\n"
    "v_mfma_f32_16x16x32_bf16 v[126:129], a[96:99], v[64:67], v[126:129]\n"
    "ds_read_b128 v[80:83], v7 offset:1024\n"
    "s_waitcnt lgkmcnt(3)\n"
    "v_mfma_f32_16x16x32_bf16 v[130:133], a[96:99], v[68:71], v[130:133]\n"
    "ds_read_b128 v[84:87], v7 offset:21504\n"
    "buffer_load_dwordx4 a[40:43], v1, s[8:11], s52 offen offset:2048 sc1\n"
    "s_waitcnt lgkmcnt(3)\n"
    "v_mfma_f32_16x16x32_bf16 v[118:121], a[100:103], v[72:75], v[118:121]\n"
    "ds_read_b128 v[56:59], v6 offset:2048\n"
    "s_waitcnt lgkmcnt(3)\n"
    "v_mfma_f32_16x16x32_bf16 v[122:125], a[100:103], v[76:79], v[122:125]\n"
    "ds_read_b128 v[60:63], v6 offset:22528\n"
    "s_waitcnt lgkmcnt(3)\n"
    "v_mfma_f32_16x16x32_bf16 v[126:129], a[100:103], v[80:83], v[126:129]\n"
    "ds_read_b128 v[64:67], v7 offset:2048\n"
    "buffer_load_dwordx4 a[44:47], v1, s[8:11], s52 offen offset:3072 sc1\n"
    "s_waitcnt lgkmcnt(3)\n"
    "v_mfma_f32_16x16x32_bf16 v[130:133], a[100:103], v[84:87], v[130:133]\n"
    "ds_read_b128 v[68:71], v7 offset:22528\n"
    "s_waitcnt lgkmcnt(3)\n"
    "v_mfma_f32_16x16x32_bf16 v[118:121], a[104:107], v[56:59], v[118:121]\n"
    "ds_read_b128 v[72:75], v6 offset:3072\n"
    "s_waitcnt lgkmcnt(3)\n"
    "v_mfma_f32_16x16x32_bf16 v[122:125], a[104:107], v[60:63], v[122:125]\n"
    "ds_read_b128 v[76:79], v6 offset:23552\n"
    "s_waitcnt lgkmcnt(3)\n"
    "v_mfma_f32_16x16x32_bf16 v[126:129], a[104:107], v[64:67], v[126:129]\n"
    "ds_read_b128 v[80:83], v7 offset:3072\n"
    "s_add_u32 s52, s28, 0x3000\n"
    "buffer_load_dwordx4 a[48:51], v1, s[8:11], s52 offen offset:0 sc1\n"
    "s_waitcnt lgkmcnt(3)\n"
    "v_mfma_f32_16x16x32_bf16 v[130:133], a[104:107], v[68:71], v[130:133]\n"
    "ds_read_b128 v[84:87], v7 offset:23552\n"
    "s_waitcnt vmcnt(14)\n"
    "s_barrier\n"
    "s_waitcnt lgkmcnt(3)\n"
    "v_mfma_f32_16x16x32_bf16 v[118:121], a[108:111], v[72:75], v[118:121]\n"
    "ds_read_b128 v[56:59], v6 offset:4096\n"
    "buffer_load_dwordx4 a[52:55], v1, s[8:11], s52 offen offset:1024 sc1\n"
    "s_waitcnt lgkmcnt(3)\n"
    "v_mfma_f32_16x16x32_bf16 v[122:125], a[108:111], v[76:79], v[122:125]\n"
    "ds_read_b128 v[60:63], v6 offset:24576\n"
    "s_waitcnt lgkmcnt(3)\n"
    "v_mfma_f32_16x16x32_bf16 v[126:129], a[108:111], v[80:83], v[126:129]\n"
    "ds_read_b128 v[64:67], v7 offset:4096\n"
    "s_waitcnt lgkmcnt(3)\n"
    "v_mfma_f32_16x16x32_bf16 v[130:133], a[108:111], v[84:87], v[130:133]\n"
    "ds_read_b128 v[68:71], v7 offset:24576\n"
    "buffer_load_dwordx4 a[56:59], v1, s[8:11], s52 offen offset:2048 sc1\n"
    "s_waitcnt lgkmcnt(3)\n"
    "v_mfma_f32_16x16x32_bf16 v[118:121], a[112:115], v[56:59], v[118:121]\n"
    "ds_read_b128 v[72:75], v6 offset:5120\n"
    "s_waitcnt lgkmcnt(3)\n"
    "v_mfma_f32_16x16x32_bf16 v[122:125], a[112:115], v[60:63], v[122:125]\n"
    "ds_read_b128 v[76:79], v6 offset:25600\n"
    "s_waitcnt lgkmcnt(3)\n"
    "v_mfma_f32_16x16x32_bf16 v[126:129], a[112:115], v[64:67], v[126:129]\n"
    "ds_read_b128 v[80:83], v7 offset:5120\n"
    "buffer_load_dwordx4 a[60:63], v1, s[8:11], s52 offen offset:3072 sc1\n"
    "s_waitcnt lgkmcnt(3)\n"
    "v_mfma_f32_16x16x32_bf16 v[130:133], a[112:115], v[68:71], v[130:133]\n"
    "ds_read_b128 v[84:87], v7 offset:25600\n"
    "s_waitcnt lgkmcnt(3)\n"
    "v_mfma_f32_16x16x32_bf16 v[118:121], a[116:119], v[72:75], v[118:121]\n"
    "ds_read_b128 v[56:59], v6 offset:6144\n"
    "s_waitcnt lgkmcnt(3)\n"
    "v_mfma_f32_16x16x32_bf16 v[122:125], a[116:119], v[76:79], v[122:125]\n"
    "ds_read_b128 v[60:63], v6 offset:26624\n"
    "s_waitcnt lgkmcnt(3)\n"
    "v_mfma_f32_16x16x32_bf16 v[126:129], a[116:119], v[80:83], v[126:129]\n"
    "ds_read_b128 v[64:67], v7 offset:6144\n"
    "s_add_u32 s52, s28, 0x4000\n"
    "buffer_load_dwordx4 a[64:67], v1, s[8:11], s52 offen offset:0 sc1\n"
    "s_waitcnt lgkmcnt(3)\n"
    "v_mfma_f32_16x16x32_bf16 v[130:133], a[116:119], v[84:87], v[130:133]\n"
    "ds_read_b128 v[68:71], v7 offset:26624\n"
    "s_waitcnt lgkmcnt(3)\n"
    "v_mfma_f32_16x16x32_bf16 v[118:121], a[120:123], v[56:59], v[118:121]\n"
    "ds_read_b128 v[72:75], v6 offset:7168\n"
    "s_waitcnt lgkmcnt(3)\n"
    "v_mfma_f32_16x16x32_bf16 v[122:125], a[120:123], v[60:63], v[122:125]\n"
    "ds_read_b128 v[76:79], v6 offset:27648\n"
    "buffer_load_dwordx4 a[68:71], v1, s[8:11], s52 offen offset:1024 sc1\n"
    "s_waitcnt lgkmcnt(3)\n"
    "v_mfma_f32_16x16x32_bf16 v[126:129], a[120:123], v[64:67], v[126:129]\n"
    "ds_read_b128 v[80:83], v7 offset:7168\n"
    "s_waitcnt lgkmcnt(3)\n"
    "v_mfma_f32_16x16x32_bf16 v[130:133], a[120:123], v[68:71], v[130:133]\n"
    "ds_read_b128 v[84:87], v7 offset:27648\n"
    "s_waitcnt vmcnt(14)\n"
    "s_barrier\n"
    "s_waitcnt lgkmcnt(3)\n"
    "v_mfma_f32_16x16x32_bf16 v[118:121], a[124:127], v[72:75], v[118:121]\n"
    "ds_read_b128 v[56:59], v6 offset:8192\n"
    "buffer_load_dwordx4 a[72:75], v1, s[8:11], s52 offen offset:2048 sc1\n"
    "s_waitcnt lgkmcnt(3)\n"
    "v_mfma_f32_16x16x32_bf16 v[122:125], a[124:127], v[76:79], v[122:125]\n"
    "ds_read_b128 v[60:63], v6 offset:28672\n"
    "s_waitcnt lgkmcnt(3)\n"
    "v_mfma_f32_16x16x32_bf16 v[126:129], a[124:127], v[80:83], v[126:129]\n"
    "ds_read_b128 v[64:67], v7 offset:8192\n"
    "buffer_load_dwordx4 a[76:79], v1, s[8:11], s52 offen offset:3072 sc1\n"
    "s_waitcnt lgkmcnt(3)\n"
    "v_mfma_f32_16x16x32_bf16 v[130:133], a[124:127], v[84:87], v[130:133]\n"
    "ds_read_b128 v[68:71], v7 offset:28672\n"
    "s_waitcnt lgkmcnt(3)\n"
    "v_mfma_f32_16x16x32_bf16 v[118:121], a[128:131], v[56:59], v[118:121]\n"
    "ds_read_b128 v[72:75], v6 offset:9216\n"
    "s_waitcnt lgkmcnt(3)\n"
    "v_mfma_f32_16x16x32_bf16 v[122:125], a[128:131], v[60:63], v[122:125]\n"
    "ds_read_b128 v[76:79], v6 offset:29696\n"
    "s_waitcnt lgkmcnt(3)\n"
    "v_mfma_f32_16x16x32_bf16 v[126:129], a[128:131], v[64:67], v[126:129]\n"
    "ds_read_b128 v[80:83], v7 offset:9216\n"
    "s_add_u32 s52, s28, 0x5000\n"
    "buffer_load_dwordx4 a[80:83], v1, s[8:11], s52 offen offset:0 sc1\n"
    "s_waitcnt lgkmcnt(3)\n"
    "v_mfma_f32_16x16x32_bf16 v[130:133], a[128:131], v[68:71], v[130:133]\n"
    "ds_read_b128 v[84:87], v7 offset:29696\n"
    "s_waitcnt lgkmcnt(3)\n"
    "v_mfma_f32_16x16x32_bf16 v[118:121], a[132:135], v[72:75], v[118:121]\n"
    "ds_read_b128 v[56:59], v6 offset:10240\n"
    "s_waitcnt lgkmcnt(3)\n"
    "v_mfma_f32_16x16x32_bf16 v[122:125], a[132:135], v[76:79], v[122:125]\n"
    "ds_read_b128 v[60:63], v6 offset:30720\n"
    "buffer_load_dwordx4 a[84:87], v1, s[8:11], s52 offen offset:1024 sc1\n"
    "s_waitcnt lgkmcnt(3)\n"
    "v_mfma_f32_16x16x32_bf16 v[126:129], a[132:135], v[80:83], v[126:129]\n"
    "ds_read_b128 v[64:67], v7 offset:10240\n"
    "s_waitcnt lgkmcnt(3)\n"
    "v_mfma_f32_16x16x32_bf16 v[130:133], a[132:135], v[84:87], v[130:133]\n"
    "ds_read_b128 v[68:71], v7 offset:30720\n"
    "s_waitcnt lgkmcnt(3)\n"
    "v_mfma_f32_16x16x32_bf16 v[118:121], a[136:139], v[56:59], v[118:121]\n"
    "ds_read_b128 v[72:75], v6 offset:11264\n"
    "s_waitcnt lgkmcnt(3)\n"
    "v_mfma_f32_16x16x32_bf16 v[122:125], a[136:139], v[60:63], v[122:125]\n"
    "ds_read_b128 v[76:79], v6 offset:31744\n"
    "buffer_load_dwordx4 a[88:91], v1, s[8:11], s52 offen offset:2048 sc1\n"
    "s_waitcnt lgkmcnt(3)\n"
    "v_mfma_f32_16x16x32_bf16 v[126:129], a[136:139], v[64:67], v[126:129]\n"
    "ds_read_b128 v[80:83], v7 offset:11264\n"
    "s_waitcnt lgkmcnt(3)\n"
    "v_mfma_f32_16x16x32_bf16 v[130:133], a[136:139], v[68:71], v[130:133]\n"
    "ds_read_b128 v[84:87], v7 offset:31744\n"
    "s_waitcnt vmcnt(17)\n"
    "s_barrier\n"
    "s_waitcnt lgkmcnt(3)\n"
    "v_mfma_f32_16x16x32_bf16 v[118:121], a[140:143], v[72:75], v[118:121]\n"
    "ds_read_b128 v[56:59], v6 offset:12288\n"
    "buffer_load_dwordx4 a[92:95], v1, s[8:11], s52 offen offset:3072 sc1\n"
    "s_waitcnt lgkmcnt(3)\n"
    "v_mfma_f32_16x16x32_bf16 v[122:125], a[140:143], v[76:79], v[122:125]\n"
    "ds_read_b128 v[60:63], v6 offset:32768\n"
    "s_waitcnt lgkmcnt(3)\n"
    "v_mfma_f32_16x16x32_bf16 v[126:129], a[140:143], v[80:83], v[126:129]\n"
    "ds_read_b128 v[64:67], v7 offset:12288\n"
    "s_add_u32 s52, s28, 0x6000\n"
    "buffer_load_dwordx4 a[96:99], v1, s[8:11], s52 offen offset:0 sc1\n"
    "s_waitcnt lgkmcnt(3)\n"
    "v_mfma_f32_16x16x32_bf16 v[130:133], a[140:143], v[84:87], v[130:133]\n"
    "ds_read_b128 v[68:71], v7 offset:32768\n"
    "s_waitcnt lgkmcnt(3)\n"
    "v_mfma_f32_16x16x32_bf16 v[118:121], a[144:147], v[56:59], v[118:121]\n"
    "ds_read_b128 v[72:75], v6 offset:13312\n"
    "s_waitcnt lgkmcnt(3)\n"
    "v_mfma_f32_16x16x32_bf16 v[122:125], a[144:147], v[60:63], v[122:125]\n"
    "ds_read_b128 v[76:79], v6 offset:33792\n"
    "buffer_load_dwordx4 a[100:103], v1, s[8:11], s52 offen offset:1024 sc1\n"
    "s_waitcnt lgkmcnt(3)\n"
    "v_mfma_f32_16x16x32_bf16 v[126:129], a[144:147], v[64:67], v[126:129]\n"
    "ds_read_b128 v[80:83], v7 offset:13312\n"
    "s_waitcnt lgkmcnt(3)\n"
    "v_mfma_f32_16x16x32_bf16 v[130:133], a[144:147], v[68:71], v[130:133]\n"
    "ds_read_b128 v[84:87], v7 offset:33792\n"
    "s_waitcnt vmcnt(17)\n"
    "s_barrier\n"
    "s_waitcnt lgkmcnt(3)\n"
    "v_mfma_f32_16x16x32_bf16 v[118:121], a[148:151], v[72:75], v[118:121]\n"
    "ds_read_b128 v[56:59], v6 offset:14336\n"
    "s_waitcnt lgkmcnt(3)\n"
    "v_mfma_f32_16x16x32_bf16 v[122:125], a[148:151], v[76:79], v[122:125]\n"
    "ds_read_b128 v[60:63], v6 offset:34816\n"
    "s_waitcnt lgkmcnt(3)\n"
    "v_mfma_f32_16x16x32_bf16 v[126:129], a[148:151], v[80:83], v[126:129]\n"
    "ds_read_b128 v[64:67], v7 offset:14336\n"
    "s_waitcnt lgkmcnt(3)\n"
    "v_mfma_f32_16x16x32_bf16 v[130:133], a[148:151], v[84:87], v[130:133]\n"
    "ds_read_b128 v[68:71], v7 offset:34816\n"
    "s_waitcnt lgkmcnt(3)\n"
    "v_mfma_f32_16x16x32_bf16 v[118:121], a[152:155], v[56:59], v[118:121]\n"
    "ds_read_b128 v[72:75], v6 offset:15360\n"
    "s_waitcnt lgkmcnt(3)\n"
    "v_mfma_f32_16x16x32_bf16 v[122:125], a[152:155], v[60:63], v[122:125]\n"
    "ds_read_b128 v[76:79], v6 offset:35840\n"
    "s_waitcnt lgkmcnt(3)\n"
    "v_mfma_f32_16x16x32_bf16 v[126:129], a[152:155], v[64:67], v[126:129]\n"
    "ds_read_b128 v[80:83], v7 offset:15360\n"
    "s_waitcnt lgkmcnt(3)\n"
    "v_mfma_f32_16x16x32_bf16 v[130:133], a[152:155], v[68:71], v[130:133]\n"
    "ds_read_b128 v[84:87], v7 offset:35840\n"
    "s_waitcnt lgkmcnt(3)\n"
    "v_mfma_f32_16x16x32_bf16 v[118:121], a[156:159], v[72:75], v[118:121]\n"
    "s_waitcnt lgkmcnt(2)\n"
    "v_mfma_f32_16x16x32_bf16 v[122:125], a[156:159], v[76:79], v[122:125]\n"
    "s_waitcnt lgkmcnt(1)\n"
    "v_mfma_f32_16x16x32_bf16 v[126:129], a[156:159], v[80:83], v[126:129]\n"
    "s_waitcnt lgkmcnt(0)\n"
    "v_mfma_f32_16x16x32_bf16 v[130:133], a[156:159], v[84:87], v[130:133]\n"
    "s_nop 1\n"
    "v_pk_fma_f32 v[8:9], v[110:111], v[118:119], v[8:9] op_sel:[1,0,0]\n"
    "v_pk_fma_f32 v[10:11], v[110:111], v[120:121], v[10:11] op_sel:[1,0,0]\n"
    "v_pk_fma_f32 v[12:13], v[112:113], v[122:123], v[12:13] op_sel:[1,0,0]\n"
    "v_pk_fma_f32 v[14:15], v[112:113], v[124:125], v[14:15] op_sel:[1,0,0]\n"
    "v_pk_fma_f32 v[16:17], v[114:115], v[126:127], v[16:17] op_sel:[1,0,0]\n"
    "v_pk_fma_f32 v[18:19], v[114:115], v[128:129], v[18:19] op_sel:[1,0,0]\n"
    "v_pk_fma_f32 v[20:21], v[116:117], v[130:131], v[20:21] op_sel:[1,0,0]\n"
    "v_pk_fma_f32 v[22:23], v[116:117], v[132:133], v[22:23] op_sel:[1,0,0]\n"
    "v_cvt_pk_bf16_f32 v134, v8, v9\n"
    "v_cvt_pk_bf16_f32 v135, v10, v11\n"
    "v_cvt_pk_bf16_f32 v136, v12, v13\n"
    "v_cvt_pk_bf16_f32 v137, v14, v15\n"
    "v_cvt_pk_bf16_f32 v138, v16, v17\n"
    "v_cvt_pk_bf16_f32 v139, v18, v19\n"
    "v_cvt_pk_bf16_f32 v140, v20, v21\n"
    "v_cvt_pk_bf16_f32 v141, v22, v23\n"
    "s_cmp_eq_u32 s23, 1\n"
    "s_cbranch_scc1 Llast_42_%=\n"
    "v_dot2_f32_bf16 v142, s42, v134, 0\n"
    "v_dot2_f32_bf16 v146, v134, v134, 0\n"
    "v_dot2_f32_bf16 v143, s42, v136, 0\n"
    "v_dot2_f32_bf16 v147, v136, v136, 0\n"
    "v_dot2_f32_bf16 v144, s42, v138, 0\n"
    "v_dot2_f32_bf16 v148, v138, v138, 0\n"
    "v_dot2_f32_bf16 v145, s42, v140, 0\n"
    "v_dot2_f32_bf16 v149, v140, v140, 0\n"
    "v_dot2_f32_bf16 v142, s42, v135, v142\n"
    "v_dot2_f32_bf16 v146, v135, v135, v146\n"
    "v_dot2_f32_bf16 v143, s42, v137, v143\n"
    "v_dot2_f32_bf16 v147, v137, v137, v147\n"
    "v_dot2_f32_bf16 v144, s42, v139, v144\n"
    "v_dot2_f32_bf16 v148, v139, v139, v148\n"
    "v_dot2_f32_bf16 v145, s42, v141, v145\n"
    "v_dot2_f32_bf16 v149, v141, v141, v149\n"
    "s_add_u32 s54, s76, s40\n"
    "s_add_u32 s55, s54, 0x7000\n"
    "s_add_u32 s56, s76, s41\n"
    "v_permlane16_swap_b32 v134, v136\n"
    "v_permlane16_swap_b32 v135, v137\n"
    "buffer_store_dwordx4 v[134:137], v2, s[8:11], s54 offen sc1\n"
    "v_permlane16_swap_b32 v138, v140\n"
    "v_permlane16_swap_b32 v139, v141\n"
    "buffer_store_dwordx4 v[138:141], v2, s[8:11], s55 offen sc1\n"
    "v_permlane16_swap_b32 v142, v143\n"
    "v_permlane16_swap_b32 v144, v145\n"
    "v_permlane16_swap_b32 v146, v147\n"
    "v_permlane16_swap_b32 v148, v149\n"
    "v_add_f32 v142, v142, v143\n"
    "v_add_f32 v144, v144, v145\n"
    "v_add_f32 v146, v146, v147\n"
    "v_add_f32 v148, v148, v149\n"
    "v_permlane32_swap_b32 v142, v144\n"
    "s_nop 0\n"
    "v_permlane32_swap_b32 v146, v148\n"
    "v_add_f32 v142, v142, v144\n"
    "v_add_f32 v143, v146, v148\n"
    "buffer_store_dwordx2 v[142:143], v3, s[8:11], s56 offen sc1\n"
    "; signal 5: every wave has its write-through stores acknowledged; barrier; one wave stores the flag line\n"
    "buffer_load_dwordx4 a[104:107], v1, s[8:11], s52 offen offset:2048 sc1\n"
    "buffer_load_dwordx4 a[108:111], v1, s[8:11], s52 offen offset:3072 sc1\n"
    "s_waitcnt vmcnt(2)\n"
    "s_barrier\n"
    "s_cmp_lg_u32 s20, 0\n"
    "s_cbranch_scc1 Lskip_43_%=\n"
    "s_add_u32 s60, s39, 0x28000\n"
    "s_add_u32 s60, s60, s38\n"
    "s_mov_b64 exec, 0xff\n"
    "buffer_store_dwordx4 v[36:39], v1, s[8:11], s60 offen sc1\n"
    "s_mov_b64 exec, -1\n"
    "Lskip_43_%=:\n"
    "v_permlane16_swap_b32 v134, v136\n"
    "v_permlane16_swap_b32 v135, v137\n"
    "v_permlane16_swap_b32 v138, v140\n"
    "v_permlane16_swap_b32 v139, v141\n"
    "v_lshlrev_b32 v8, 16, v134\n"
    "v_and_b32 v9, s51, v134\n"
    "v_lshlrev_b32 v10, 16, v135\n"
    "v_and_b32 v11, s51, v135\n"
    "v_lshlrev_b32 v12, 16, v136\n"
    "v_and_b32 v13, s51, v136\n"
    "v_lshlrev_b32 v14, 16, v137\n"
    "v_and_b32 v15, s51, v137\n"
    "v_lshlrev_b32 v16, 16, v138\n"
    "v_and_b32 v17, s51, v138\n"
    "v_lshlrev_b32 v18, 16, v139\n"
    "v_and_b32 v19, s51, v139\n"
    "v_lshlrev_b32 v20, 16, v140\n"
    "v_and_b32 v21, s51, v140\n"
    "v_lshlrev_b32 v22, 16, v141\n"
    "v_and_b32 v23, s51, v141\n"
    "s_sleep 1\n"
    "s_sleep 1\n"
    "s_sleep 1\n"
    "s_sleep 1\n"
    "s_sleep 1\n"
    "s_sleep 1\n"
    "s_sleep 1\n"
    "s_sleep 1\n"
    "s_sleep 1\n"
    "s_sleep 1\n"
    "s_add_u32 s71, s38, 0x28000\n"
    "buffer_load_dwordx2 v[46:47], v4, s[8:11], s71 offen sc1\n"
    "s_mov_b32 s23, 1\n"
    "s_add_u32 s74, s74, 0x480000\n"
    "s_add_u32 s75, s75, 0x480000\n"
    "s_add_u32 s35, s35, 0x400000\n"
    "s_add_u32 s36, s36, 0x200000\n"
    "s_add_u32 s37, s37, 0x700000\n"
    "s_add_u32 s38, s38, 0x30000\n"
    "s_add_u32 s66, s66, s79\n"
    "s_add_u32 s67, s67, s80\n"
    "s_add_u32 s68, s68, s81\n"
    "s_add_u32 s69, s69, s82\n"
    "s_add_u32 s70, s70, s83\n"
    "Ljoin_44_%=:\n"
    "s_add_u32 s65, s74, s43\n"
    "s_lshr_b32 s73, s43, 2\n"
    "s_add_u32 s73, s73, s74\n"
    "s_add_u32 s73, s73, 0xe000\n"
    "s_waitcnt vmcnt(0)\n"
    "v_cmp_ne_u64 vcc, s[26:27], v[46:47]\n"
    "s_nop 0\n"
    "s_cbranch_vccnz Lspin_50_%=\n"
    "Lgo_51_%=:\n"
    "; K and V projections\n"
    "s_add_u32 s53, s65, 0x0\n"
    "s_add_u32 m0, s43, 0x0\n"
    "s_nop 0\n"
    "buffer_load_dwordx4 v1, s[8:11], s53 offen offset:0 sc1 lds\n"
    "buffer_load_dwordx4 v1, s[8:11], s53 offen offset:1024 sc1 lds\n"
    "buffer_load_dwordx4 v1, s[8:11], s53 offen offset:2048 sc1 lds\n"
    "buffer_load_dwordx4 v1, s[8:11], s53 offen offset:3072 sc1 lds\n"
    "s_add_u32 s53, s65, 0x1000\n"
    "s_add_u32 m0, s43, 0x1000\n"
    "s_nop 0\n"
    "buffer_load_dwordx4 v1, s[8:11], s53 offen offset:0 sc1 lds\n"
    "buffer_load_dwordx4 v1, s[8:11], s53 offen offset:1024 sc1 lds\n"
    "buffer_load_dwordx4 v1, s[8:11], s53 offen offset:2048 sc1 lds\n"
    "buffer_load_dwordx2 v[88:89], v3, s[8:11], s73 offen offset:0 sc1\n"
    "buffer_load_dwordx2 v[90:91], v3, s[8:11], s73 offen offset:512 sc1\n"
    "buffer_load_dwordx2 v[92:93], v3, s[8:11], s73 offen offset:1024 sc1\n"
    "buffer_load_dwordx2 v[94:95], v3, s[8:11], s73 offen offset:1536 sc1\n"
    "buffer_load_dwordx2 v[96:97], v3, s[8:11], s73 offen offset:2048 sc1\n"
    "buffer_load_dwordx2 v[98:99], v3, s[8:11], s73 offen offset:2560 sc1\n"
    "s_waitcnt vmcnt(11)\n"
    "s_barrier\n"
    "ds_read_b128 v[56:59], v1 offset:0\n"
    "ds_read_b128 v[60:63], v1 offset:14336\n"
    "ds_read_b128 v[64:67], v1 offset:28672\n"
    "ds_read_b128 v[68:71], v1 offset:43008\n"
    "buffer_load_dwordx2 v[100:101], v3, s[8:11], s73 offen offset:3072 sc1\n"
    "s_waitcnt lgkmcnt(3)\n"
    "v_mfma_f32_16x16x32_bf16 v[118:121], a[0:3], v[56:59], 0\n"
    "ds_read_b128 v[72:75], v1 offset:1024\n"
    "v_mfma_f32_16x16x32_bf16 v[134:137], v[56:59], a[4:7], 0\n"
    "buffer_load_dwordx4 v1, s[8:11], s53 offen offset:3072 sc1 lds\n"
    "s_waitcnt lgkmcnt(3)\n"
    "v_mfma_f32_16x16x32_bf16 v[122:125], a[0:3], v[60:63], 0\n"
    "ds_read_b128 v[76:79], v1 offset:15360\n"
    "v_mfma_f32_16x16x32_bf16 v[138:141], v[60:63], a[4:7], 0\n"
    "s_waitcnt lgkmcnt(3)\n"
    "v_mfma_f32_16x16x32_bf16 v[126:129], a[0:3], v[64:67], 0\n"
    "ds_read_b128 v[80:83], v1 offset:29696\n"
    "v_mfma_f32_16x16x32_bf16 v[142:145], v[64:67], a[4:7], 0\n"
    "s_add_u32 s53, s65, 0x2000\n"
    "s_add_u32 m0, s43, 0x2000\n"
    "s_nop 0\n"
    "buffer_load_dwordx4 v1, s[8:11], s53 offen offset:0 sc1 lds\n"
    "s_waitcnt lgkmcnt(3)\n"
    "v_mfma_f32_16x16x32_bf16 v[130:133], a[0:3], v[68:71], 0\n"
    "ds_read_b128 v[84:87], v1 offset:44032\n"
    "v_mfma_f32_16x16x32_bf16 v[146:149], v[68:71], a[4:7], 0\n"
    "buffer_load_dwordx4 v1, s[8:11], s53 offen offset:1024 sc1 lds\n"
    "s_waitcnt vmcnt(13)\n"
    "s_barrier\n"
    "s_waitcnt lgkmcnt(3)\n"
    "v_mfma_f32_16x16x32_bf16 v[118:121], a[8:11], v[72:75], v[118:121]\n"
    "ds_read_b128 v[56:59], v1 offset:2048\n"
    "v_mfma_f32_16x16x32_bf16 v[134:137], v[72:75], a[12:15], v[134:137]\n"
    "buffer_load_dwordx4 v1, s[8:11], s53 offen offset:2048 sc1 lds\n"
    "s_waitcnt lgkmcnt(3)\n"
    "v_mfma_f32_16x16x32_bf16 v[122:125], a[8:11], v[76:79], v[122:125]\n"
    "ds_read_b128 v[60:63], v1 offset:16384\n"
    "v_mfma_f32_16x16x32_bf16 v[138:141], v[76:79], a[12:15], v[138:141]\n"
    "s_waitcnt lgkmcnt(3)\n"
    "v_mfma_f32_16x16x32_bf16 v[126:129], a[8:11], v[80:83], v[126:129]\n"
    "ds_read_b128 v[64:67], v1 offset:30720\n"
    "v_mfma_f32_16x16x32_bf16 v[142:145], v[80:83], a[12:15], v[142:145]\n"
    "buffer_load_dwordx4 v1, s[8:11], s53 offen offset:3072 sc1 lds\n"
    "s_waitcnt lgkmcnt(3)\n"
    "v_mfma_f32_16x16x32_bf16 v[130:133], a[8:11], v[84:87], v[130:133]\n"
    "ds_read_b128 v[68:71], v1 offset:45056\n"
    "v_mfma_f32_16x16x32_bf16 v[146:149], v[84:87], a[12:15], v[146:149]\n"
    "s_add_u32 s53, s65, 0x3000\n"
    "s_add_u32 m0, s43, 0x3000\n"
    "s_nop 0\n"
    "buffer_load_dwordx4 v1, s[8:11], s53 offen offset:0 sc1 lds\n"
    "s_waitcnt lgkmcnt(3)\n"
    "v_mfma_f32_16x16x32_bf16 v[118:121], a[16:19], v[56:59], v[118:121]\n"
    "ds_read_b128 v[72:75], v1 offset:3072\n"
    "v_mfma_f32_16x16x32_bf16 v[134:137], v[56:59], a[20:23], v[134:137]\n"
    "s_waitcnt lgkmcnt(3)\n"
    "v_mfma_f32_16x16x32_bf16 v[122:125], a[16:19], v[60:63], v[122:125]\n"
    "ds_read_b128 v[76:79], v1 offset:17408\n"
    "v_mfma_f32_16x16x32_bf16 v[138:141], v[60:63], a[20:23], v[138:141]\n"
    "buffer_load_dwordx4 v1, s[8:11], s53 offen offset:1024 sc1 lds\n"
    "s_waitcnt lgkmcnt(3)\n"
    "v_mfma_f32_16x16x32_bf16 v[126:129], a[16:19], v[64:67], v[126:129]\n"
    "ds_read_b128 v[80:83], v1 offset:31744\n"
    "v_mfma_f32_16x16x32_bf16 v[142:145], v[64:67], a[20:23], v[142:145]\n"
    "s_waitcnt lgkmcnt(3)\n"
    "v_mfma_f32_16x16x32_bf16 v[130:133], a[16:19], v[68:71], v[130:133]\n"
    "ds_read_b128 v[84:87], v1 offset:46080\n"
    "v_mfma_f32_16x16x32_bf16 v[146:149], v[68:71], a[20:23], v[146:149]\n"
    "s_add_u32 s52, s28, 0x7000\n"
    "buffer_load_dwordx4 a[112:115], v1, s[8:11], s52 offen offset:0 sc1\n"
    "s_waitcnt vmcnt(15)\n"
    "s_barrier\n"
    "s_waitcnt lgkmcnt(3)\n"
    "v_mfma_f32_16x16x32_bf16 v[118:121], a[24:27], v[72:75], v[118:121]\n"
    "ds_read_b128 v[56:59], v1 offset:4096\n"
    "v_mfma_f32_16x16x32_bf16 v[134:137], v[72:75], a[28:31], v[134:137]\n"
    "s_waitcnt lgkmcnt(3)\n"
    "v_mfma_f32_16x16x32_bf16 v[122:125], a[24:27], v[76:79], v[122:125]\n"
    "ds_read_b128 v[60:63], v1 offset:18432\n"
    "v_mfma_f32_16x16x32_bf16 v[138:141], v[76:79], a[28:31], v[138:141]\n"
    "buffer_load_dwordx4 a[116:119], v1, s[8:11], s52 offen offset:1024 sc1\n"
    "s_waitcnt lgkmcnt(3)\n"
    "v_mfma_f32_16x16x32_bf16 v[126:129], a[24:27], v[80:83], v[126:129]\n"
    "ds_read_b128 v[64:67], v1 offset:32768\n"
    "v_mfma_f32_16x16x32_bf16 v[142:145], v[80:83], a[28:31], v[142:145]\n"
    "s_waitcnt lgkmcnt(3)\n"
    "v_mfma_f32_16x16x32_bf16 v[130:133], a[24:27], v[84:87], v[130:133]\n"
    "ds_read_b128 v[68:71], v1 offset:47104\n"
    "v_mfma_f32_16x16x32_bf16 v[146:149], v[84:87], a[28:31], v[146:149]\n"
    "buffer_load_dwordx4 a[120:123], v1, s[8:11], s52 offen offset:2048 sc1\n"
    "s_waitcnt lgkmcnt(3)\n"
    "v_mfma_f32_16x16x32_bf16 v[118:121], a[32:35], v[56:59], v[118:121]\n"
    "ds_read_b128 v[72:75], v1 offset:5120\n"
    "v_mfma_f32_16x16x32_bf16 v[134:137], v[56:59], a[36:39], v[134:137]\n"
    "s_waitcnt lgkmcnt(3)\n"
    "v_mfma_f32_16x16x32_bf16 v[122:125], a[32:35], v[60:63], v[122:125]\n"
    "ds_read_b128 v[76:79], v1 offset:19456\n"
    "v_mfma_f32_16x16x32_bf16 v[138:141], v[60:63], a[36:39], v[138:141]\n"
    "buffer_load_dwordx4 a[124:127], v1, s[8:11], s52 offen offset:3072 sc1\n"
    "s_waitcnt lgkmcnt(3)\n"
    "v_mfma_f32_16x16x32_bf16 v[126:129], a[32:35], v[64:67], v[126:129]\n"
    "ds_read_b128 v[80:83], v1 offset:33792\n"
    "v_mfma_f32_16x16x32_bf16 v[142:145], v[64:67], a[36:39], v[142:145]\n"
    "s_waitcnt lgkmcnt(3)\n"
    "v_mfma_f32_16x16x32_bf16 v[130:133], a[32:35], v[68:71], v[130:133]\n"
    "ds_read_b128 v[84:87], v1 offset:48128\n"
    "v_mfma_f32_16x16x32_bf16 v[146:149], v[68:71], a[36:39], v[146:149]\n"
    "s_add_u32 s52, s28, 0x8000\n"
    "buffer_load_dwordx4 a[128:131], v1, s[8:11], s52 offen offset:0 sc1\n"
    "s_waitcnt lgkmcnt(3)\n"
    "v_mfma_f32_16x16x32_bf16 v[118:121], a[40:43], v[72:75], v[118:121]\n"
    "ds_read_b128 v[56:59], v1 offset:6144\n"
    "v_mfma_f32_16x16x32_bf16 v[134:137], v[72:75], a[44:47], v[134:137]\n"
    "s_waitcnt lgkmcnt(3)\n"
    "v_mfma_f32_16x16x32_bf16 v[122:125], a[40:43], v[76:79], v[122:125]\n"
    "ds_read_b128 v[60:63], v1 offset:20480\n"
    "v_mfma_f32_16x16x32_bf16 v[138:141], v[76:79], a[44:47], v[138:141]\n"
    "buffer_load_dwordx4 a[132:135], v1, s[8:11], s52 offen offset:1024 sc1\n"
    "s_waitcnt lgkmcnt(3)\n"
    "v_mfma_f32_16x16x32_bf16 v[126:129], a[40:43], v[80:83], v[126:129]\n"
    "ds_read_b128 v[64:67], v1 offset:34816\n"
    "v_mfma_f32_16x16x32_bf16 v[142:145], v[80:83], a[44:47], v[142:145]\n"
    "buffer_load_dwordx4 a[136:139], v1, s[8:11], s52 offen offset:2048 sc1\n"
    "s_waitcnt lgkmcnt(3)\n"
    "v_mfma_f32_16x16x32_bf16 v[130:133], a[40:43], v[84:87], v[130:133]\n"
    "ds_read_b128 v[68:71], v1 offset:49152\n"
    "v_mfma_f32_16x16x32_bf16 v[146:149], v[84:87], a[44:47], v[146:149]\n"
    "s_waitcnt vmcnt(12)\n"
    "s_barrier\n"
    "s_waitcnt lgkmcnt(3)\n"
    "v_mfma_f32_16x16x32_bf16 v[118:121], a[48:51], v[56:59], v[118:121]\n"
    "ds_read_b128 v[72:75], v1 offset:7168\n"
    "v_mfma_f32_16x16x32_bf16 v[134:137], v[56:59], a[52:55], v[134:137]\n"
    "v_add_f32 v88, v88, v90\n"
    "buffer_load_dwordx4 a[140:143], v1, s[8:11], s52 offen offset:3072 sc1\n"
    "s_waitcnt lgkmcnt(3)\n"
    "v_mfma_f32_16x16x32_bf16 v[122:125], a[48:51], v[60:63], v[122:125]\n"
    "ds_read_b128 v[76:79], v1 offset:21504\n"
    "v_add_f32 v89, v89, v91\n"
    "v_mfma_f32_16x16x32_bf16 v[138:141], v[60:63], a[52:55], v[138:141]\n"
    "v_add_f32 v92, v92, v94\n"
    "s_add_u32 s52, s28, 0x9000\n"
    "buffer_load_dwordx4 a[144:147], v1, s[8:11], s52 offen offset:0 sc1\n"
    "s_waitcnt lgkmcnt(3)\n"
    "v_mfma_f32_16x16x32_bf16 v[126:129], a[48:51], v[64:67], v[126:129]\n"
    "ds_read_b128 v[80:83], v1 offset:35840\n"
    "v_add_f32 v93, v93, v95\n"
    "v_mfma_f32_16x16x32_bf16 v[142:145], v[64:67], a[52:55], v[142:145]\n"
    "v_add_f32 v96, v96, v98\n"
    "s_waitcnt lgkmcnt(3)\n"
    "v_mfma_f32_16x16x32_bf16 v[130:133], a[48:51], v[68:71], v[130:133]\n"
    "ds_read_b128 v[84:87], v1 offset:50176\n"
    "v_add_f32 v97, v97, v99\n"
    "v_mfma_f32_16x16x32_bf16 v[146:149], v[68:71], a[52:55], v[146:149]\n"
    "v_add_f32 v88, v88, v92\n"
    "buffer_load_dwordx4 a[148:151], v1, s[8:11], s52 offen offset:1024 sc1\n"
    "s_waitcnt lgkmcnt(3)\n"
    "v_mfma_f32_16x16x32_bf16 v[118:121], a[56:59], v[72:75], v[118:121]\n"
    "ds_read_b128 v[56:59], v1 offset:8192\n"
    "v_add_f32 v89, v89, v93\n"
    "v_mfma_f32_16x16x32_bf16 v[134:137], v[72:75], a[60:63], v[134:137]\n"
    "v_add_f32 v96, v96, v100\n"
    "s_waitcnt lgkmcnt(3)\n"
    "v_mfma_f32_16x16x32_bf16 v[122:125], a[56:59], v[76:79], v[122:125]\n"
    "ds_read_b128 v[60:63], v1 offset:22528\n"
    "v_add_f32 v97, v97, v101\n"
    "v_mfma_f32_16x16x32_bf16 v[138:141], v[76:79], a[60:63], v[138:141]\n"
    "v_add_f32 v88, v88, v96\n"
    "buffer_load_dwordx4 a[152:155], v1, s[8:11], s52 offen offset:2048 sc1\n"
    "s_waitcnt lgkmcnt(3)\n"
    "v_mfma_f32_16x16x32_bf16 v[126:129], a[56:59], v[80:83], v[126:129]\n"
    "ds_read_b128 v[64:67], v1 offset:36864\n"
    "v_add_f32 v89, v89, v97\n"
    "v_mfma_f32_16x16x32_bf16 v[142:145], v[80:83], a[60:63], v[142:145]\n"
    "ds_write_b64 v43, v[88:89]\n"
    "buffer_load_dwordx4 a[156:159], v1, s[8:11], s52 offen offset:3072 sc1\n"
    "s_waitcnt lgkmcnt(4)\n"
    "v_mfma_f32_16x16x32_bf16 v[130:133], a[56:59], v[84:87], v[130:133]\n"
    "ds_read_b128 v[68:71], v1 offset:51200\n"
    "v_mfma_f32_16x16x32_bf16 v[146:149], v[84:87], a[60:63], v[146:149]\n"
    "s_waitcnt vmcnt(12)\n"
    "s_waitcnt lgkmcnt(1)\n"
    "s_barrier\n"
    "v_mfma_f32_16x16x32_bf16 v[118:121], a[64:67], v[56:59], v[118:121]\n"
    "ds_read_b128 v[72:75], v1 offset:9216\n"
    "v_and_b32 v88, 0xffffffe0, v43\n"
    "v_mfma_f32_16x16x32_bf16 v[134:137], v[56:59], a[68:71], v[134:137]\n"
    "ds_read_b128 v[102:105], v88\n"
    "s_add_u32 s52, s28, 0xa000\n"
    "buffer_load_dwordx4 a[160:163], v1, s[8:11], s52 offen offset:0 sc1\n"
    "v_mfma_f32_16x16x32_bf16 v[122:125], a[64:67], v[60:63], v[122:125]\n"
    "ds_read_b128 v[76:79], v1 offset:23552\n"
    "ds_read_b128 v[106:109], v88 offset:16\n"
    "v_mfma_f32_16x16x32_bf16 v[138:141], v[60:63], a[68:71], v[138:141]\n"
    "buffer_load_dwordx4 a[164:167], v1, s[8:11], s52 offen offset:1024 sc1\n"
    "v_mfma_f32_16x16x32_bf16 v[126:129], a[64:67], v[64:67], v[126:129]\n"
    "ds_read_b128 v[80:83], v1 offset:37888\n"
    "v_mfma_f32_16x16x32_bf16 v[142:145], v[64:67], a[68:71], v[142:145]\n"
    "s_waitcnt lgkmcnt(5)\n"
    "v_mfma_f32_16x16x32_bf16 v[130:133], a[64:67], v[68:71], v[130:133]\n"
    "ds_read_b128 v[84:87], v1 offset:52224\n"
    "v_mfma_f32_16x16x32_bf16 v[146:149], v[68:71], a[68:71], v[146:149]\n"
    "s_waitcnt lgkmcnt(5)\n"
    "v_mfma_f32_16x16x32_bf16 v[118:121], a[72:75], v[72:75], v[118:121]\n"
    "ds_read_b128 v[56:59], v1 offset:10240\n"
    "s_waitcnt lgkmcnt(5)\n"
    "v_add_f32 v102, v102, v104\n"
    "v_mfma_f32_16x16x32_bf16 v[134:137], v[72:75], a[76:79], v[134:137]\n"
    "v_add_f32 v103, v103, v105\n"
    "s_waitcnt lgkmcnt(4)\n"
    "v_mfma_f32_16x16x32_bf16 v[122:125], a[72:75], v[76:79], v[122:125]\n"
    "ds_read_b128 v[60:63], v1 offset:24576\n"
    "s_waitcnt lgkmcnt(4)\n"
    "v_add_f32 v106, v106, v108\n"
    "v_mfma_f32_16x16x32_bf16 v[138:141], v[76:79], a[76:79], v[138:141]\n"
    "v_add_f32 v107, v107, v109\n"
    "s_waitcnt lgkmcnt(3)\n"
    "v_mfma_f32_16x16x32_bf16 v[126:129], a[72:75], v[80:83], v[126:129]\n"
    "ds_read_b128 v[64:67], v1 offset:38912\n"
    "v_add_f32 v102, v102, v106\n"
    "v_mfma_f32_16x16x32_bf16 v[142:145], v[80:83], a[76:79], v[142:145]\n"
    "v_add_f32 v103, v103, v107\n"
    "s_waitcnt lgkmcnt(3)\n"
    "v_mfma_f32_16x16x32_bf16 v[130:133], a[72:75], v[84:87], v[130:133]\n"
    "ds_read_b128 v[68:71], v1 offset:53248\n"
    "v_mul_f32 v102, 0x3b124925, v102\n"
    "v_mfma_f32_16x16x32_bf16 v[146:149], v[84:87], a[76:79], v[146:149]\n"
    "v_mul_f32 v103, 0x3b124925, v103\n"
    "s_waitcnt lgkmcnt(3)\n"
    "v_mfma_f32_16x16x32_bf16 v[118:121], a[80:83], v[56:59], v[118:121]\n"
    "ds_read_b128 v[72:75], v1 offset:11264\n"
    "v_fma_f32 v103, -v102, v102, v103\n"
    "v_mfma_f32_16x16x32_bf16 v[134:137], v[56:59], a[84:87], v[134:137]\n"
    "v_max_f32 v103, 0, v103\n"
    "s_waitcnt lgkmcnt(3)\n"
    "v_mfma_f32_16x16x32_bf16 v[122:125], a[80:83], v[60:63], v[122:125]\n"
    "ds_read_b128 v[76:79], v1 offset:25600\n"
    "v_add_f32 v103, 0x3727c5ac, v103\n"
    "v_mfma_f32_16x16x32_bf16 v[138:141], v[60:63], a[84:87], v[138:141]\n"
    "v_rsq_f32 v103, v103\n"
    "s_waitcnt lgkmcnt(3)\n"
    "v_mfma_f32_16x16x32_bf16 v[126:129], a[80:83], v[64:67], v[126:129]\n"
    "ds_read_b128 v[80:83], v1 offset:39936\n"
    "v_mfma_f32_16x16x32_bf16 v[142:145], v[64:67], a[84:87], v[142:145]\n"
    "ds_write2st64_b32 v49, v102, v103 offset1:1\n"
    "s_waitcnt lgkmcnt(4)\n"
    "v_mfma_f32_16x16x32_bf16 v[130:133], a[80:83], v[68:71], v[130:133]\n"
    "ds_read_b128 v[84:87], v1 offset:54272\n"
    "ds_read2_b32 v[110:111], v50 offset0:0 offset1:64\n"
    "v_mfma_f32_16x16x32_bf16 v[146:149], v[68:71], a[84:87], v[146:149]\n"
    "ds_read2_b32 v[112:113], v50 offset0:16 offset1:80\n"
    "s_waitcnt lgkmcnt(6)\n"
    "v_mfma_f32_16x16x32_bf16 v[118:121], a[88:91], v[72:75], v[118:121]\n"
    "ds_read_b128 v[56:59], v1 offset:12288\n"
    "ds_read2_b32 v[114:115], v50 offset0:32 offset1:96\n"
    "v_mfma_f32_16x16x32_bf16 v[134:137], v[72:75], a[92:95], v[134:137]\n"
    "ds_read2_b32 v[116:117], v50 offset0:48 offset1:112\n"
    "s_waitcnt lgkmcnt(8)\n"
    "v_mfma_f32_16x16x32_bf16 v[122:125], a[88:91], v[76:79], v[122:125]\n"
    "ds_read_b128 v[60:63], v1 offset:26624\n"
    "ds_read_b128 v[166:169], v51 offset:0\n"
    "v_mfma_f32_16x16x32_bf16 v[138:141], v[76:79], a[92:95], v[138:141]\n"
    "ds_read_b128 v[170:173], v51 offset:256\n"
    "s_waitcnt lgkmcnt(10)\n"
    "v_mfma_f32_16x16x32_bf16 v[126:129], a[88:91], v[80:83], v[126:129]\n"
    "ds_read_b128 v[64:67], v1 offset:40960\n"
    "ds_read_b128 v[174:177], v51 offset:64\n"
    "v_mfma_f32_16x16x32_bf16 v[142:145], v[80:83], a[92:95], v[142:145]\n"
    "ds_read_b128 v[178:181], v51 offset:320\n"
    "s_waitcnt lgkmcnt(11)\n"
    "v_mfma_f32_16x16x32_bf16 v[130:133], a[88:91], v[84:87], v[130:133]\n"
    "ds_read_b128 v[68:71], v1 offset:55296\n"
    "ds_read_b128 v[182:185], v51 offset:128\n"
    "v_mfma_f32_16x16x32_bf16 v[146:149], v[84:87], a[92:95], v[146:149]\n"
    "ds_read_b128 v[186:189], v51 offset:384\n"
    "s_waitcnt lgkmcnt(11)\n"
    "v_mfma_f32_16x16x32_bf16 v[118:121], a[96:99], v[56:59], v[118:121]\n"
    "ds_read_b128 v[72:75], v1 offset:13312\n"
    "ds_read_b128 v[190:193], v51 offset:192\n"
    "v_mfma_f32_16x16x32_bf16 v[134:137], v[56:59], a[100:103], v[134:137]\n"
    "ds_read_b128 v[194:197], v51 offset:448\n"
    "s_waitcnt lgkmcnt(11)\n"
    "v_mfma_f32_16x16x32_bf16 v[122:125], a[96:99], v[60:63], v[122:125]\n"
    "ds_read_b128 v[76:79], v1 offset:27648\n"
    "v_mfma_f32_16x16x32_bf16 v[138:141], v[60:63], a[100:103], v[138:141]\n"
    "s_waitcnt lgkmcnt(9)\n"
    "v_mfma_f32_16x16x32_bf16 v[126:129], a[96:99], v[64:67], v[126:129]\n"
    "ds_read_b128 v[80:83], v1 offset:41984\n"
    "v_mfma_f32_16x16x32_bf16 v[142:145], v[64:67], a[100:103], v[142:145]\n"
    "s_waitcnt lgkmcnt(7)\n"
    "v_mfma_f32_16x16x32_bf16 v[130:133], a[96:99], v[68:71], v[130:133]\n"
    "ds_read_b128 v[84:87], v1 offset:56320\n"
    "v_mfma_f32_16x16x32_bf16 v[146:149], v[68:71], a[100:103], v[146:149]\n"
    "s_waitcnt lgkmcnt(5)\n"
    "v_mfma_f32_16x16x32_bf16 v[118:121], a[104:107], v[72:75], v[118:121]\n"
    "ds_read_b128 v[56:59], v1 offset:0\n"
    "v_mfma_f32_16x16x32_bf16 v[134:137], v[72:75], a[108:111], v[134:137]\n"
    "s_waitcnt lgkmcnt(3)\n"
    "v_mfma_f32_16x16x32_bf16 v[122:125], a[104:107], v[76:79], v[122:125]\n"
    "ds_read_b128 v[60:63], v1 offset:14336\n"
    "v_mfma_f32_16x16x32_bf16 v[138:141], v[76:79], a[108:111], v[138:141]\n"
    "s_waitcnt lgkmcnt(3)\n"
    "v_mfma_f32_16x16x32_bf16 v[126:129], a[104:107], v[80:83], v[126:129]\n"
    "ds_read_b128 v[64:67], v1 offset:28672\n"
    "v_mfma_f32_16x16x32_bf16 v[142:145], v[80:83], a[108:111], v[142:145]\n"
    "s_waitcnt lgkmcnt(3)\n"
    "v_mfma_f32_16x16x32_bf16 v[130:133], a[104:107], v[84:87], v[130:133]\n"
    "ds_read_b128 v[68:71], v1 offset:43008\n"
    "v_mfma_f32_16x16x32_bf16 v[146:149], v[84:87], a[108:111], v[146:149]\n"
    "s_nop 7\n"
    "s_branch Llayer_19_%=\n"
    "Lspin_17_%=:\n"
    "s_mov_b32 s64, 0x80000\n"
    "Lspin_17_%=l:\n"
    "buffer_load_dwordx2 v[46:47], v105, s[8:11], s71 offen sc1\n"
    "s_waitcnt vmcnt(0)\n"
    "v_cmp_ne_u64 vcc, s[26:27], v[46:47]\n"
    "s_nop 1\n"
    "s_cbranch_vccz Lgo_18_%=\n"
    "s_sub_u32 s64, s64, 1\n"
    "s_cbranch_scc0 Lspin_17_%=l\n"
    "s_branch Lgo_18_%=\n"
    "Lspin_21_%=:\n"
    "s_mov_b32 s64, 0x80000\n"
    "Lspin_21_%=l:\n"
    "buffer_load_dwordx2 v[46:47], v35, s[8:11], s71 offen sc1\n"
    "s_waitcnt vmcnt(0)\n"
    "v_cmp_ne_u64 vcc, s[26:27], v[46:47]\n"
    "s_nop 1\n"
    "s_cbranch_vccz Lgo_22_%=\n"
    "s_sub_u32 s64, s64, 1\n"
    "s_cbranch_scc0 Lspin_21_%=l\n"
    "s_branch Lgo_22_%=\n"
    "Lspin_25_%=:\n"
    "s_mov_b32 s64, 0x80000\n"
    "Lspin_25_%=l:\n"
    "buffer_load_dwordx2 v[46:47], v40, s[8:11], s71 offen sc1\n"
    "s_waitcnt vmcnt(0)\n"
    "v_cmp_ne_u64 vcc, s[26:27], v[46:47]\n"
    "s_nop 1\n"
    "s_cbranch_vccz Lgo_26_%=\n"
    "s_sub_u32 s64, s64, 1\n"
    "s_cbranch_scc0 Lspin_25_%=l\n"
    "s_branch Lgo_26_%=\n"
    "Lspin_29_%=:\n"
    "s_mov_b32 s64, 0x80000\n"
    "Lspin_29_%=l:\n"
    "buffer_load_dwordx2 v[46:47], v5, s[8:11], s71 offen sc1\n"
    "s_waitcnt vmcnt(0)\n"
    "v_cmp_ne_u64 vcc, s[26:27], v[46:47]\n"
    "s_nop 1\n"
    "s_cbranch_vccz Lgo_30_%=\n"
    "s_sub_u32 s64, s64, 1\n"
    "s_cbranch_scc0 Lspin_29_%=l\n"
    "s_branch Lgo_30_%=\n"
    "Lspin_33_%=:\n"
    "s_mov_b32 s64, 0x80000\n"
    "Lspin_33_%=l:\n"
    "buffer_load_dwordx2 v[46:47], v4, s[8:11], s71 offen sc1\n"
    "s_waitcnt vmcnt(0)\n"
    "v_cmp_ne_u64 vcc, s[26:27], v[46:47]\n"
    "s_nop 1\n"
    "s_cbranch_vccz Lgo_34_%=\n"
    "s_sub_u32 s64, s64, 1\n"
    "s_cbranch_scc0 Lspin_33_%=l\n"
    "s_branch Lgo_34_%=\n"
    "Lspin_37_%=:\n"
    "s_mov_b32 s64, 0x80000\n"
    "Lspin_37_%=l:\n"
    "buffer_load_dwordx2 v[46:47], v5, s[8:11], s71 offen sc1\n"
    "s_waitcnt vmcnt(0)\n"
    "v_cmp_ne_u64 vcc, s[26:27], v[46:47]\n"
    "s_nop 1\n"
    "s_cbranch_vccz Lgo_38_%=\n"
    "s_sub_u32 s64, s64, 1\n"
    "s_cbranch_scc0 Lspin_37_%=l\n"
    "s_branch Lgo_38_%=\n"
    "Lspin_40_%=:\n"
    "s_mov_b32 s64, 0x80000\n"
    "Lspin_40_%=l:\n"
    "buffer_load_dwordx2 v[46:47], v5, s[8:11], s71 offen sc1\n"
    "s_waitcnt vmcnt(0)\n"
    "v_cmp_ne_u64 vcc, s[26:27], v[46:47]\n"
    "s_nop 1\n"
    "s_cbranch_vccz Lgo_41_%=\n"
    "s_sub_u32 s64, s64, 1\n"
    "s_cbranch_scc0 Lspin_40_%=l\n"
    "s_branch Lgo_41_%=\n"
    "Llast_42_%=:\n"
    "s_nop 1\n"
    "v_permlane16_swap_b32 v134, v136\n"
    "v_permlane16_swap_b32 v135, v137\n"
    "v_permlane16_swap_b32 v138, v140\n"
    "v_permlane16_swap_b32 v139, v141\n"
    "v_and_b32 v118, 31, v0\n"
    "v_mul_u32_u24 v118, 0x380, v118\n"
    "v_lshrrev_b32 v119, 5, v0\n"
    "v_lshl_add_u32 v118, v119, 4, v118\n"
    "s_mul_i32 s54, s21, 0xe000\n"
    "s_lshr_b32 s55, s40, 4\n"
    "s_add_u32 s54, s54, s55\n"
    "s_add_u32 s55, s54, 0x7000\n"
    "buffer_store_dwordx4 v[134:137], v118, s[16:19], s54 offen\n"
    "buffer_store_dwordx4 v[138:141], v118, s[16:19], s55 offen\n"
    "s_branch Lend_%=\n"
    "Lnog_36_%=:\n"
    "s_nop 1\n"
    "s_waitcnt vmcnt(0)\n"
    "; signal 4: every wave has its write-through stores acknowledged; barrier; one wave stores the flag line\n"
    "s_barrier\n"
    "s_cmp_lg_u32 s20, 0\n"
    "s_cbranch_scc1 Lskip_45_%=\n"
    "s_add_u32 s60, s39, 0x20000\n"
    "s_add_u32 s60, s60, s38\n"
    "s_mov_b64 exec, 0xff\n"
    "buffer_store_dwordx4 v[36:39], v1, s[8:11], s60 offen sc1\n"
    "s_mov_b64 exec, -1\n"
    "Lskip_45_%=:\n"
    "s_cmp_eq_u32 s23, 1\n"
    "s_cbranch_scc1 Lend_%=\n"
    "s_cmp_lg_u32 s24, 56\n"
    "s_cbranch_scc1 Lskip_46_%=\n"
    "s_cmp_lg_u32 s20, 0\n"
    "s_cbranch_scc1 Lskip_47_%=\n"
    "v_lshlrev_b32 v56, 7, v0\n"
    "s_mov_b32 s64, 0x80000\n"
    "Lep_48_%=:\n"
    "s_mov_b32 s54, 0x68000\n"
    "buffer_load_dwordx2 v[58:59], v56, s[8:11], s54 offen sc1\n"
    "s_mov_b32 s54, 0x6a000\n"
    "buffer_load_dwordx2 v[60:61], v56, s[8:11], s54 offen sc1\n"
    "s_mov_b32 s54, 0x6c000\n"
    "buffer_load_dwordx2 v[62:63], v56, s[8:11], s54 offen sc1\n"
    "s_mov_b32 s54, 0x6e000\n"
    "buffer_load_dwordx2 v[64:65], v56, s[8:11], s54 offen sc1\n"
    "s_sub_u32 s64, s64, 1\n"
    "s_cbranch_scc1 Lepx_49_%=\n"
    "s_waitcnt vmcnt(0)\n"
    "v_cmp_ne_u64 vcc, s[26:27], v[58:59]\n"
    "s_nop 0\n"
    "s_cbranch_vccnz Lep_48_%=\n"
    "v_cmp_ne_u64 vcc, s[26:27], v[60:61]\n"
    "s_nop 0\n"
    "s_cbranch_vccnz Lep_48_%=\n"
    "v_cmp_ne_u64 vcc, s[26:27], v[62:63]\n"
    "s_nop 0\n"
    "s_cbranch_vccnz Lep_48_%=\n"
    "v_cmp_ne_u64 vcc, s[26:27], v[64:65]\n"
    "s_nop 0\n"
    "s_cbranch_vccnz Lep_48_%=\n"
    "s_mov_b32 s54, 0x7c000\n"
    "s_mov_b64 exec, 0xff\n"
    "buffer_store_dwordx4 v[36:39], v1, s[8:11], s54 offen sc1\n"
    "s_mov_b64 exec, -1\n"
    "Lepx_49_%=:\n"
    "s_waitcnt vmcnt(0)\n"
    "Lskip_47_%=:\n"
    "Lskip_46_%=:\n"
    "buffer_load_dwordx4 a[0:3], v1, s[8:11], s28 offen offset:0 sc1\n"
    "buffer_load_dwordx4 a[4:7], v1, s[8:11], s28 offen offset:1024 sc1\n"
    "buffer_load_dwordx4 a[8:11], v1, s[8:11], s28 offen offset:2048 sc1\n"
    "buffer_load_dwordx4 a[12:15], v1, s[8:11], s28 offen offset:3072 sc1\n"
    "s_add_u32 s52, s28, 0x1000\n"
    "buffer_load_dwordx4 a[16:19], v1, s[8:11], s52 offen offset:0 sc1\n"
    "buffer_load_dwordx4 a[20:23], v1, s[8:11], s52 offen offset:1024 sc1\n"
    "buffer_load_dwordx4 a[24:27], v1, s[8:11], s52 offen offset:2048 sc1\n"
    "buffer_load_dwordx4 a[28:31], v1, s[8:11], s52 offen offset:3072 sc1\n"
    "s_add_u32 s52, s28, 0x2000\n"
    "buffer_load_dwordx4 a[32:35], v1, s[8:11], s52 offen offset:0 sc1\n"
    "buffer_load_dwordx4 a[36:39], v1, s[8:11], s52 offen offset:1024 sc1\n"
    "buffer_load_dwordx4 a[40:43], v1, s[8:11], s52 offen offset:2048 sc1\n"
    "buffer_load_dwordx4 a[44:47], v1, s[8:11], s52 offen offset:3072 sc1\n"
    "s_add_u32 s52, s28, 0x3000\n"
    "buffer_load_dwordx4 a[48:51], v1, s[8:11], s52 offen offset:0 sc1\n"
    "buffer_load_dwordx4 a[52:55], v1, s[8:11], s52 offen offset:1024 sc1\n"
    "buffer_load_dwordx4 a[56:59], v1, s[8:11], s52 offen offset:2048 sc1\n"
    "buffer_load_dwordx4 a[60:63], v1, s[8:11], s52 offen offset:3072 sc1\n"
    "s_add_u32 s52, s28, 0x4000\n"
    "buffer_load_dwordx4 a[64:67], v1, s[8:11], s52 offen offset:0 sc1\n"
    "buffer_load_dwordx4 a[68:71], v1, s[8:11], s52 offen offset:1024 sc1\n"
    "buffer_load_dwordx4 a[72:75], v1, s[8:11], s52 offen offset:2048 sc1\n"
    "buffer_load_dwordx4 a[76:79], v1, s[8:11], s52 offen offset:3072 sc1\n"
    "s_add_u32 s52, s28, 0x5000\n"
    "buffer_load_dwordx4 a[80:83], v1, s[8:11], s52 offen offset:0 sc1\n"
    "buffer_load_dwordx4 a[84:87], v1, s[8:11], s52 offen offset:1024 sc1\n"
    "buffer_load_dwordx4 a[88:91], v1, s[8:11], s52 offen offset:2048 sc1\n"
    "buffer_load_dwordx4 a[92:95], v1, s[8:11], s52 offen offset:3072 sc1\n"
    "s_add_u32 s52, s28, 0x6000\n"
    "buffer_load_dwordx4 a[96:99], v1, s[8:11], s52 offen offset:0 sc1\n"
    "buffer_load_dwordx4 a[100:103], v1, s[8:11], s52 offen offset:1024 sc1\n"
    "buffer_load_dwordx4 a[104:107], v1, s[8:11], s52 offen offset:2048 sc1\n"
    "buffer_load_dwordx4 a[108:111], v1, s[8:11], s52 offen offset:3072 sc1\n"
    "s_add_u32 s71, s38, 0x28000\n"
    "buffer_load_dwordx2 v[46:47], v4, s[8:11], s71 offen sc1\n"
    "s_mov_b32 s23, 1\n"
    "s_add_u32 s74, s74, 0x480000\n"
    "s_add_u32 s75, s75, 0x480000\n"
    "s_add_u32 s35, s35, 0x400000\n"
    "s_add_u32 s36, s36, 0x200000\n"
    "s_add_u32 s37, s37, 0x700000\n"
    "s_add_u32 s38, s38, 0x30000\n"
    "s_add_u32 s66, s66, s79\n"
    "s_add_u32 s67, s67, s80\n"
    "s_add_u32 s68, s68, s81\n"
    "s_add_u32 s69, s69, s82\n"
    "s_add_u32 s70, s70, s83\n"
    "s_branch Ljoin_44_%=\n"
    "Lspin_50_%=:\n"
    "s_mov_b32 s64, 0x80000\n"
    "Lspin_50_%=l:\n"
    "buffer_load_dwordx2 v[46:47], v4, s[8:11], s71 offen sc1\n"
    "s_waitcnt vmcnt(0)\n"
    "v_cmp_ne_u64 vcc, s[26:27], v[46:47]\n"
    "s_nop 1\n"
    "s_cbranch_vccz Lgo_51_%=\n"
    "s_sub_u32 s64, s64, 1\n"
    "s_cbranch_scc0 Lspin_50_%=l\n"
    "s_branch Lgo_51_%=\n"
    "Lend_%=:\n"
    : "+v"(tid)
    : "s"(x), "s"(wqkv), "s"(wo), "s"(w1), "s"(w2), "s"(ws), "s"(out), "s"(blockIdx.x)
    : "v1", "v2", "v3", "v4", "v5", "v6", "v7", "v8", "v9", "v10", "v11", "v12", "v13", "v14", "v15", "v16", "v17", "v18", "v19", "v20", "v21", "v22", "v23", "v24", "v25", "v26", "v27", "v28", "v29", "v30", "v31", "v32", "v33", "v34", "v35", "v36", "v37", "v38", "v39", "v40", "v41", "v42", "v43", "v44", "v45", "v46", "v47", "v48", "v49", "v50", "v51", "v52", "v53", "v54", "v55", "v56", "v57", "v58", "v59", "v60", "v61", "v62", "v63", "v64", "v65", "v66", "v67", "v68", "v69", "v70", "v71", "v72", "v73", "v74", "v75", "v76", "v77", "v78", "v79", "v80", "v81", "v82", "v83", "v84", "v85", "v86", "v87", "v88", "v89", "v90", "v91", "v92", "v93", "v94", "v95", "v96", "v97", "v98", "v99", "v100", "v101", "v102", "v103", "v104", "v105", "v106", "v107", "v108", "v109", "v110", "v111", "v112", "v113", "v114", "v115", "v116", "v117", "v118", "v119", "v120", "v121", "v122", "v123", "v124", "v125", "v126", "v127", "v128", "v129", "v130", "v131", "v132", "v133", "v134", "v135", "v136", "v137", "v138", "v139", "v140", "v141", "v142", "v143", "v144", "v145", "v146", "v147", "v148", "v149", "v150", "v151", "v152", "v153", "v154", "v155", "v156", "v157", "v158", "v159", "v160", "v161", "v162", "v163", "v164", "v165", "v166", "v167", "v168", "v169", "v170", "v171", "v172", "v173", "v174", "v175", "v176", "v177", "v178", "v179", "v180", "v181", "v182", "v183", "v184", "v185", "v186", "v187", "v188", "v189", "v190", "v191", "v192", "v193", "v194", "v195", "v196", "v197", "v198", "v199", "v200", "v201", "v202", "v203", "v204", "v205", "v206", "v207", "v208", "v209", "v210", "v211", "v212", "v213", "v214", "v215", "v216", "v217", "v218", "v219", "v220", "v221", "v222", "v223", "v224", "v225", "v226", "v227", "v228", "v229", "v230", "v231", "v232", "v233", "v234", "v235", "v236", "v237", "v238", "v239", "v240", "v241", "v242", "v243", "v244", "v245", "v246", "v247", "v248", "v249", "v250", "v251", "v252", "v253", "v254", "v255", "a0", "a1", "a2", "a3", "a4", "a5", "a6", "a7", "a8", "a9", "a10", "a11", "a12", "a13", "a14", "a15", "a16", "a17", "a18", "a19", "a20", "a21", "a22", "a23", "a24", "a25", "a26", "a27", "a28", "a29", "a30", "a31", "a32", "a33", "a34", "a35", "a36", "a37", "a38", "a39", "a40", "a41", "a42", "a43", "a44", "a45", "a46", "a47", "a48", "a49", "a50", "a51", "a52", "a53", "a54", "a55", "a56", "a57", "a58", "a59", "a60", "a61", "a62", "a63", "a64", "a65", "a66", "a67", "a68", "a69", "a70", "a71", "a72", "a73", "a74", "a75", "a76", "a77", "a78", "a79", "a80", "a81", "a82", "a83", "a84", "a85", "a86", "a87", "a88", "a89", "a90", "a91", "a92", "a93", "a94", "a95", "a96", "a97", "a98", "a99", "a100", "a101", "a102", "a103", "a104", "a105", "a106", "a107", "a108", "a109", "a110", "a111", "a112", "a113", "a114", "a115", "a116", "a117", "a118", "a119", "a120", "a121", "a122", "a123", "a124", "a125", "a126", "a127", "a128", "a129", "a130", "a131", "a132", "a133", "a134", "a135", "a136", "a137", "a138", "a139", "a140", "a141", "a142", "a143", "a144", "a145", "a146", "a147", "a148", "a149", "a150", "a151", "a152", "a153", "a154", "a155", "a156", "a157", "a158", "a159", "a160", "a161", "a162", "a163", "a164", "a165", "a166", "a167", "a168", "a169", "a170", "a171", "a172", "a173", "a174", "a175", "a176", "a177", "a178", "a179", "a180", "a181", "a182", "a183", "a184", "a185", "a186", "a187", "a188", "a189", "a190", "a191", "a192", "a193", "a194", "a195", "a196", "a197", "a198", "a199", "a200", "a201", "a202", "a203", "a204", "a205", "a206", "a207", "a208", "a209", "a210", "a211", "a212", "a213", "a214", "a215", "a216", "a217", "a218", "a219", "a220", "a221", "a222", "a223", "a224", "a225", "a226", "a227", "a228", "a229", "a230", "a231", "a232", "a233", "a234", "a235", "a236", "a237", "a238", "a239", "a240", "a241", "a242", "a243", "a244", "a245", "a246", "a247", "a248", "a249", "a250", "a251", "a252", "a253", "a254", "a255", "s8", "s9", "s10", "s11", "s12", "s13", "s14", "s15", "s16", "s17", "s18", "s19", "s20", "s21", "s22", "s23", "s24", "s25", "s26", "s27", "s28", "s29", "s30", "s31", "s35", "s36", "s37", "s38", "s39", "s40", "s41", "s42", "s43", "s44", "s45", "s46", "s47", "s48", "s49", "s50", "s51", "s52", "s53", "s54", "s55", "s56", "s57", "s58", "s59", "s60", "s61", "s62", "s63", "s64", "s65", "s66", "s67", "s68", "s69", "s70", "s71", "s72", "s73", "s74", "s75", "s76", "s77", "s78", "s79", "s80", "s81", "s82", "s83", "s84", "s85", "s86", "s87", "s88", "s89", "s90", "s91", "vcc", "scc", "memory");
}

extern "C" void kernel_launch(void* const* d_in, const int* in_sizes, int n_in, void* d_out, int out_size, void* d_ws, size_t ws_size,
                              hipStream_t stream) {
  (void)hipFuncSetAttribute((const void*)k_layers, hipFuncAttributeMaxDynamicSharedMemorySize, LDS_BYTES);
  k_layers<<<256, 256, LDS_BYTES, stream>>>(d_in[0], d_in[1], d_in[2], d_in[4], d_in[5], d_ws, d_out);
}
```
